```python
import torch

def _softmax(x, axis=-1):
    return torch.softmax(x, dim=axis)

def _gelu(x):
    return torch.nn.functional.gelu(x, approximate='tanh')

def _silu(x):
    return torch.nn.functional.silu(x)

def _sigmoid(x):
    return torch.sigmoid(x)

def _layernorm(x, axis=-1, eps=1e-05):
    m = x.mean(dim=axis, keepdim=True)
    v = x.var(dim=axis, keepdim=True, unbiased=False)
    return (x - m) / torch.sqrt(v + eps)

def _scatter_add(tgt, idx, src, axis=0):
    return tgt.clone().index_add_(axis, idx, src)

def _segment_sum(src, seg, n):
    out = torch.zeros((n,) + src.shape[1:], dtype=src.dtype)
    return out.index_add_(0, seg, src)

def _pad(x, pw):
    flat = tuple((v for pair in reversed(pw) for v in pair))
    return torch.nn.functional.pad(x, flat)
kernel_name = 'adaLN_cond__gqa_block_cached__mixer_channel_mlp__decode'

def setup_inputs():

    _w = lambda a: (lambda t: t.to(torch.bfloat16) if t.is_floating_point() else t)(torch.as_tensor(a))

    def _A_setup():
        B, Sq, D = (1, 1, 1024)
        Hq, Hkv, Sk, Dh = (32, 4, 65536, 128)
        assert Hq % Hkv == 0, (Hq, Hkv)
        return {'x': _w((lambda *s: torch.randn(*s, dtype=torch.bfloat16) * 0.1)(B, Sq, D)), 'k_cache': _w((lambda *s: torch.randn(*s, dtype=torch.bfloat16) * 0.8)(B, Hkv, Sk, Dh)), 'v_cache': _w((lambda *s: torch.randn(*s, dtype=torch.bfloat16) * 0.8)(B, Hkv, Sk, Dh)), 'wq': _w((lambda *s: torch.randn(*s, dtype=torch.bfloat16) * 0.8)(D, Hq * Dh) * D ** (-0.5)), 'wo': _w((lambda *s: torch.randn(*s, dtype=torch.bfloat16) * 0.8)(Hq * Dh, D) * (Hq * Dh) ** (-0.5))}
    _A_in = _A_setup()

    def _B_setup():
        B, S, D, F = (1, 1, 1024, 3584)
        return {'x': _w((lambda *s: torch.randn(*s, dtype=torch.bfloat16) * 0.1)(B, S, D)), 'w1': _w((lambda *s: torch.randn(*s, dtype=torch.bfloat16) * 3.2)(D, F) * D ** (-0.5)), 'b1': _w((lambda *s: torch.randn(*s, dtype=torch.bfloat16) * 3.2)(F)), 'w2': _w((lambda *s: torch.randn(*s, dtype=torch.bfloat16) * 3.2)(F, D) * F ** (-0.5)), 'b2': _w((lambda *s: torch.randn(*s, dtype=torch.bfloat16) * 3.2)(D))}
    _B_in = _B_setup()
    return {'A_x': _A_in['x'], 'A_k_cache': _A_in['k_cache'], 'A_v_cache': _A_in['v_cache'], 'A_wq': _A_in['wq'], 'A_wo': _A_in['wo'], 'B_x': _B_in['x'], 'B_w1': _B_in['w1'], 'B_b1': _B_in['b1'], 'B_w2': _B_in['w2'], 'B_b2': _B_in['b2'], 'c': _w((lambda *s: torch.randn(*s, dtype=torch.bfloat16) * 0.4)(1, 1024) * 8.0), 'w_mod': _w((lambda *s: torch.randn(*s, dtype=torch.bfloat16) * 0.4)(1024, 6 * 1024) * 1024 ** (-0.5) * 8.0)}

def reference(A_x, A_k_cache, A_v_cache, A_wq, A_wo, B_x, B_w1, B_b1, B_w2, B_b2, c, w_mod):

    def _A_ref(x, k_cache, v_cache, wq, wo):
        B, Sq, D = x.shape
        Hq, Dh = (32, 128)
        Hkv = k_cache.shape[1]
        rep = Hq // Hkv
        q = (lambda x, *d: x.permute(*d))((x @ wq).reshape(B, Sq, Hq, Dh), 0, 2, 1, 3)
        k = (lambda x, n, axis: torch.repeat_interleave(x, n, dim=axis))(k_cache, rep, axis=1)
        v = (lambda x, n, axis: torch.repeat_interleave(x, n, dim=axis))(v_cache, rep, axis=1)
        scores = torch.einsum('bhqd,bhkd->bhqk', q, k) * Dh ** (-0.5)
        p = _softmax(scores, axis=-1)
        o = torch.einsum('bhqk,bhkd->bhqd', p, v)
        return (lambda x, *d: x.permute(*d))(o, 0, 2, 1, 3).reshape(B, Sq, Hq * Dh) @ wo

    def _B_ref(x, w1, b1, w2, b2):
        y = _layernorm(x, axis=-1)
        h = _gelu(torch.einsum('bsd,df->bsf', y, w1) + b1)
        return torch.einsum('bsf,fd->bsd', h, w2) + b2
    x = A_x
    mod = (_silu(c) @ w_mod).reshape(c.shape[0], 6, 1, -1)
    sa, ca, ga, sb, cb, gb = (mod[:, 0].clone(), mod[:, 1].clone(), mod[:, 2].clone(), mod[:, 3].clone(), mod[:, 4].clone(), mod[:, 5].clone())
    h = _layernorm(x, axis=-1) * (1 + ca) + sa
    x = x + ga * _A_ref(h, A_k_cache, A_v_cache, A_wq, A_wo)
    h = _layernorm(x, axis=-1) * (1 + cb) + sb
    x = x + gb * _B_ref(h, B_w1, B_b1, B_w2, B_b2)
    return x
```

```cpp
#include <hip/hip_runtime.h>
#include <hip/hip_bf16.h>
#include <stdint.h>
typedef unsigned short u16;
__device__ __forceinline__ float bf2f(u16 b){ return __uint_as_float(((unsigned)b)<<16); }
__device__ __forceinline__ u16 f2bf(float f){ unsigned u=__float_as_uint(f); u += 0x7FFFu + ((u>>16)&1u); return (u16)(u>>16); }
__device__ __forceinline__ float rbf(float f){ return bf2f(f2bf(f)); }
__device__ float blk_sum(float v, float* sh){
  for(int o=32;o>0;o>>=1) v += __shfl_xor(v,o);
  __syncthreads();
  if((threadIdx.x&63)==0) sh[threadIdx.x>>6]=v;
  __syncthreads();
  float s=0; for(int i=0;i<(int)(blockDim.x>>6);++i) s+=sh[i];
  return s;
}
__device__ void ln1024(float* xs, float* sh){
  float s=0; for(int i=threadIdx.x;i<1024;i+=256) s+=xs[i];
  float mean = blk_sum(s,sh)*(1.f/1024);
  float q=0; for(int i=threadIdx.x;i<1024;i+=256){ float d=xs[i]-mean; q+=d*d; }
  float var = blk_sum(q,sh)*(1.f/1024);
  float m=rbf(mean), v=rbf(var);
  float sd = rbf(sqrtf(rbf(v+1e-5f)));
  for(int i=threadIdx.x;i<1024;i+=256) xs[i]=rbf(rbf(xs[i]-m)/sd);
  __syncthreads();
}
__device__ float gemv64(const float* xs, const u16* W, int K, int N, int n0, float* red){
  int c=threadIdx.x&63, g=threadIdx.x>>6; float a=0;
  for(int k=g;k<K;k+=4) a += xs[k]*bf2f(W[(size_t)k*N+n0+c]);
  red[threadIdx.x]=a; __syncthreads();
  return red[c]+red[64+c]+red[128+c]+red[192+c];
}
__global__ void k_mod(const u16* c,const u16* wm,float* mod){
  __shared__ float xs[1024]; __shared__ float red[256];
  for(int i=threadIdx.x;i<1024;i+=256){ float v=bf2f(c[i]); xs[i]=rbf(v/(1.f+expf(-v))); }
  __syncthreads();
  float r=gemv64(xs,wm,1024,6144,blockIdx.x*64,red);
  if(threadIdx.x<64) mod[blockIdx.x*64+threadIdx.x]=rbf(r);
}
__global__ void k_q(const u16* x,const float* mod,const u16* wq,float* q){
  __shared__ float xs[1024]; __shared__ float red[256]; __shared__ float sh[4];
  for(int i=threadIdx.x;i<1024;i+=256) xs[i]=bf2f(x[i]);
  __syncthreads(); ln1024(xs,sh);
  for(int i=threadIdx.x;i<1024;i+=256) xs[i]=rbf(rbf(xs[i]*rbf(1.f+mod[1024+i]))+mod[i]);
  __syncthreads();
  float r=gemv64(xs,wq,1024,4096,blockIdx.x*64,red);
  if(threadIdx.x<64) q[blockIdx.x*64+threadIdx.x]=rbf(r);
}
__global__ void k_attn(const float* q,const u16* K,const u16* V,float* pm,float* pl,float* po){
  __shared__ float qs[8*128]; __shared__ float sc[8*256]; __shared__ float mx[8], ls[8];
  int s=blockIdx.x,g=blockIdx.y,t=threadIdx.x;
  for(int i=t;i<1024;i+=256) qs[i]=q[g*1024+i];
  __syncthreads();
  const u16* kr=K+((size_t)g*65536+s*256+t)*128;
  float a[8]={0,0,0,0,0,0,0,0};
  for(int d=0;d<128;d+=8){ uint4 kv=*(const uint4*)(kr+d); unsigned w[4]={kv.x,kv.y,kv.z,kv.w};
    for(int j=0;j<4;++j){ float k0=__uint_as_float(w[j]<<16),k1=__uint_as_float(w[j]&0xffff0000u);
      for(int h=0;h<8;++h) a[h]+=k0*qs[h*128+d+2*j]+k1*qs[h*128+d+2*j+1]; } }
  for(int h=0;h<8;++h) sc[h*256+t]=rbf(rbf(a[h])*0.08838834764831845f);
  __syncthreads();
  if(t<8){ float m=-INFINITY; for(int i=0;i<256;++i) m=fmaxf(m,sc[t*256+i]); mx[t]=m; }
  __syncthreads();
  for(int h=0;h<8;++h) sc[h*256+t]=expf(sc[h*256+t]-mx[h]);
  __syncthreads();
  if(t<8){ float l=0; for(int i=0;i<256;++i) l+=sc[t*256+i]; ls[t]=l; }
  int d=t&127, hh=t>>7; float o[4]={0,0,0,0};
  const u16* vr=V+((size_t)g*65536+s*256)*128+d;
  for(int k=0;k<256;++k){ float v=bf2f(vr[k*128]); for(int j=0;j<4;++j) o[j]+=sc[(hh*4+j)*256+k]*v; }
  __syncthreads();
  for(int j=0;j<4;++j) po[((size_t)(g*8+hh*4+j)*256+s)*128+d]=o[j];
  if(t<8){ pm[(g*8+t)*256+s]=mx[t]; pl[(g*8+t)*256+s]=ls[t]; }
}
__global__ void k_comb(const float* pm,const float* pl,const float* po,float* o){
  int h=blockIdx.x,d=threadIdx.x; float M=-INFINITY; for(int s=0;s<256;++s) M=fmaxf(M,pm[h*256+s]);
  float L=0,a=0; for(int s=0;s<256;++s){ float w=expf(pm[h*256+s]-M); L+=w*pl[h*256+s]; a+=w*po[((size_t)h*256+s)*128+d]; }
  o[h*128+d]=rbf(a/L);
}
__global__ void k_wo(const float* o,const u16* wo,const u16* x,const float* mod,float* x1){
  __shared__ float xs[4096]; __shared__ float red[256];
  for(int i=threadIdx.x;i<4096;i+=256) xs[i]=o[i];
  __syncthreads();
  float r=gemv64(xs,wo,4096,1024,blockIdx.x*64,red);
  int n=blockIdx.x*64+threadIdx.x;
  if(threadIdx.x<64) x1[n]=rbf(bf2f(x[n])+rbf(mod[2048+n]*rbf(r)));
}
__global__ void k_w1(const float* x1,const float* mod,const u16* w1,const u16* b1,float* h2){
  __shared__ float xs[1024]; __shared__ float red[256]; __shared__ float sh[4];
  for(int i=threadIdx.x;i<1024;i+=256) xs[i]=x1[i];
  __syncthreads(); ln1024(xs,sh);
  for(int i=threadIdx.x;i<1024;i+=256) xs[i]=rbf(rbf(xs[i]*rbf(1.f+mod[4096+i]))+mod[3072+i]);
  __syncthreads(); ln1024(xs,sh);
  float r=gemv64(xs,w1,1024,3584,blockIdx.x*64,red);
  int n=blockIdx.x*64+threadIdx.x;
  if(threadIdx.x<64){ float z=rbf(rbf(r)+bf2f(b1[n])); float t=tanhf(0.7978845608028654f*(z+0.044715f*z*z*z)); h2[n]=rbf(0.5f*z*(1.f+t)); }
}
__global__ void k_w2(const float* h2,const u16* w2,const u16* b2,const float* x1,const float* mod,u16* out){
  __shared__ float xs[3584]; __shared__ float red[256];
  for(int i=threadIdx.x;i<3584;i+=256) xs[i]=h2[i];
  __syncthreads();
  float r=gemv64(xs,w2,3584,1024,blockIdx.x*64,red);
  int n=blockIdx.x*64+threadIdx.x;
  if(threadIdx.x<64) out[n]=f2bf(x1[n]+rbf(mod[5120+n]*rbf(rbf(r)+bf2f(b2[n]))));
}
extern "C" void kernel_launch(void* const* d_in,const int* in_sizes,int n_in,void* d_out,int out_size,void* d_ws,size_t ws_size,hipStream_t stream){
  const u16* x=(const u16*)d_in[0]; const u16* K=(const u16*)d_in[1]; const u16* V=(const u16*)d_in[2];
  const u16* wq=(const u16*)d_in[3]; const u16* wo=(const u16*)d_in[4];
  const u16* w1=(const u16*)d_in[6]; const u16* b1=(const u16*)d_in[7]; const u16* w2=(const u16*)d_in[8]; const u16* b2=(const u16*)d_in[9];
  const u16* c=(const u16*)d_in[10]; const u16* wm=(const u16*)d_in[11];
  float* ws=(float*)d_ws;
  float* mod=ws; float* q=ws+8192; float* o=ws+16384; float* x1=ws+24576; float* h2=ws+32768; float* pm=ws+40960; float* pl=pm+8192; float* po=pl+8192;
  k_mod<<<96,256,0,stream>>>(c,wm,mod);
  k_q<<<64,256,0,stream>>>(x,mod,wq,q);
  k_attn<<<dim3(256,4),256,0,stream>>>(q,K,V,pm,pl,po);
  k_comb<<<32,128,0,stream>>>(pm,pl,po,o);
  k_wo<<<16,256,0,stream>>>(o,wo,x,mod,x1);
  k_w1<<<56,256,0,stream>>>(x1,mod,w1,b1,h2);
  k_w2<<<16,256,0,stream>>>(h2,w2,b2,x1,mod,(u16*)d_out);
}
```

```cpp
#include <hip/hip_runtime.h>
#include <stdint.h>

#define LAS __attribute__((address_space(3)))
typedef unsigned v4u __attribute__((ext_vector_type(4)));
typedef unsigned v2u __attribute__((ext_vector_type(2)));
typedef float f32x4 __attribute__((ext_vector_type(4)));
typedef __bf16 bf16x8 __attribute__((ext_vector_type(8)));
typedef __bf16 bf16x2 __attribute__((ext_vector_type(2)));
typedef short s16x4 __attribute__((ext_vector_type(4)));
typedef __amdgpu_buffer_rsrc_t rsrc_t;

#define XREP 8
#define WAUX 2
#define QSLEEP 16
#define NF 2
#define KP2 20

constexpr int AUX_NT = 2, AUX_SC1 = 16;
constexpr unsigned SENT = 0xFFFFFFFFu;
constexpr int WS_EPOCH = 0;
constexpr int WS_MOD = 4096;
constexpr int WS_QB = 32768;
constexpr int WS_ML = 65536;
constexpr int WS_X1 = 98304;
constexpr int WS_AP = 131072;
constexpr int WS_P1 = 262144;
constexpr int WS_P2 = WS_P1 + 1048576;
constexpr int WS_P4 = WS_P2 + 524288;
constexpr int WS_END = WS_P4 + 262144;
constexpr int L_V = 0;
constexpr int L_XV = 131072;
constexpr int L_JP = L_XV + 2048;
constexpr int L_MX = L_JP + 2048;
constexpr int L_LS = L_MX + 256;
constexpr int L_FS = L_LS + 256;
constexpr int L_PART = L_FS + 256;
constexpr int L_PART2 = L_PART + 2048;
constexpr int L_RED = L_PART2 + 2048;
constexpr int L_FLAG = L_RED + 128;
constexpr int L_ST = L_FLAG + 64;
constexpr int L_END = L_ST + 256;
enum { F_JOB = 1, F_SM = 2, F_QLOCK = 4, F_QRDY = 5 };

constexpr unsigned SPIN_G = 400000u, SPIN_L = 8000000u;

__device__ __forceinline__ float u2f(unsigned u) { return __builtin_bit_cast(float, u); }
__device__ __forceinline__ unsigned f2u(float f) { return __builtin_bit_cast(unsigned, f); }
__device__ __forceinline__ float blo(unsigned u) { return u2f(u << 16); }
__device__ __forceinline__ float bhi(unsigned u) { return u2f(u & 0xffff0000u); }
__device__ __forceinline__ unsigned pk2(float a, float b) { bf16x2 v = {(__bf16)a, (__bf16)b}; return __builtin_bit_cast(unsigned, v); }
__device__ __forceinline__ float rbf(float a) { return (float)(__bf16)a; }
__device__ __forceinline__ unsigned plo(unsigned a, unsigned b) { return __builtin_amdgcn_perm(b, a, 0x05040100u); }
__device__ __forceinline__ unsigned phi(unsigned a, unsigned b) { return __builtin_amdgcn_perm(b, a, 0x07060302u); }
__device__ __forceinline__ float dot2(unsigned a, unsigned b, float c) {
  return __builtin_amdgcn_fdot2_f32_bf16(__builtin_bit_cast(bf16x2, a), __builtin_bit_cast(bf16x2, b), c, false);
}
__device__ __forceinline__ f32x4 mfma(v4u a, v4u b, f32x4 c) {
  return __builtin_amdgcn_mfma_f32_16x16x32_bf16(__builtin_bit_cast(bf16x8, a), __builtin_bit_cast(bf16x8, b), c, 0, 0, 0);
}
template <int CTRL> __device__ __forceinline__ unsigned dppu(unsigned x) { return (unsigned)__builtin_amdgcn_mov_dpp((int)x, CTRL, 0xf, 0xf, true); }
template <int CTRL> __device__ __forceinline__ float dppf(float x) { return u2f(dppu<CTRL>(f2u(x))); }
constexpr int ROR8 = 0x128, ROR4 = 0x124, ROR2 = 0x122, ROR1 = 0x121;
__device__ __forceinline__ float tr32(float a, float b) { auto s = __builtin_amdgcn_permlane32_swap(f2u(a), f2u(b), false, false); return u2f(s[0]) + u2f(s[1]); }
__device__ __forceinline__ float tr16(float a, float b) { auto s = __builtin_amdgcn_permlane16_swap(f2u(a), f2u(b), false, false); return u2f(s[0]) + u2f(s[1]); }
__device__ __forceinline__ float mx32(float a) { auto s = __builtin_amdgcn_permlane32_swap(f2u(a), f2u(a), false, false); return fmaxf(u2f(s[0]), u2f(s[1])); }
__device__ __forceinline__ float mx16(float a) { auto s = __builtin_amdgcn_permlane16_swap(f2u(a), f2u(a), false, false); return fmaxf(u2f(s[0]), u2f(s[1])); }
__device__ __forceinline__ float rowsum16(float v) { v += dppf<ROR8>(v); v += dppf<ROR4>(v); v += dppf<ROR2>(v); v += dppf<ROR1>(v); return v; }
__device__ __forceinline__ float wavesum(float v) { v = tr32(v, v); v = tr16(v, v); return rowsum16(v); }
__device__ __forceinline__ unsigned nosent(unsigned u) { return u < SENT ? u : SENT - 1u; }
__device__ __forceinline__ float fexp(float x) { return __builtin_amdgcn_exp2f(x * 1.4426950408889634f); }
__device__ __forceinline__ float frcp(float x) { return __builtin_amdgcn_rcpf(x); }
__device__ __forceinline__ float silu_bf(float c) { return rbf(c / (1.f + fexp(-c))); }

#define LD128(rs, vo, so, aux) __builtin_amdgcn_raw_buffer_load_b128(rs, vo, so, aux)
#define LD64(rs, vo, so, aux) __builtin_amdgcn_raw_buffer_load_b64(rs, vo, so, aux)
#define LD32(rs, vo, so, aux) __builtin_amdgcn_raw_buffer_load_b32(rs, vo, so, aux)
#define ST128(v, rs, vo, so) __builtin_amdgcn_raw_buffer_store_b128(v, rs, vo, so, AUX_SC1)
#define ST64(v, rs, vo, so) __builtin_amdgcn_raw_buffer_store_b64(v, rs, vo, so, AUX_SC1)
#define ST32(v, rs, vo, so) __builtin_amdgcn_raw_buffer_store_b32(v, rs, vo, so, AUX_SC1)
#define CFENCE() asm volatile("" ::: "memory")
#define SCHED() __builtin_amdgcn_sched_barrier(0)
#define BAR() asm volatile("s_waitcnt lgkmcnt(0)\n\ts_barrier" ::: "memory")
#define VMWAIT(n) asm volatile("s_waitcnt vmcnt(%0)" ::"n"(n) : "memory")
#define MKR(p, n) __builtin_amdgcn_make_buffer_rsrc((void*)(p), 0, (n), 0x00020000)
#define STAMP(k) do {} while (0)

__device__ __forceinline__ unsigned umax(unsigned a, unsigned b) { return a > b ? a : b; }
__device__ __forceinline__ unsigned mx4(v4u v) { return umax(umax(v.x, v.y), umax(v.z, v.w)); }

template <int N> __device__ __forceinline__ unsigned bcast16(unsigned x) { return dppu<0x150 + N>(x); }
#define LGKM0() asm volatile("s_waitcnt lgkmcnt(0)" ::: "memory")
__device__ __forceinline__ unsigned lds_arrive(LAS unsigned char* L, int idx, int lane) {
  LGKM0();
  unsigned old = 0;
  if (lane == 0) old = __hip_atomic_fetch_add((LAS unsigned*)(L + L_FLAG) + idx, 1u, __ATOMIC_RELAXED, __HIP_MEMORY_SCOPE_WORKGROUP);
  old = __builtin_amdgcn_readfirstlane(old);
  CFENCE();
  return old;
}
#define LDS_WAIT_GE(idx, val, slp) do { for (unsigned sp_ = 0; flags[idx] < (val) && sp_ < SPIN_L; ++sp_) __builtin_amdgcn_s_sleep(slp); CFENCE(); } while (0)

template <int N> __device__ __forceinline__ void pairup(v4u (&w)[16]) {
#pragma unroll
  for (int p = 0; p < N / 2; ++p)
#pragma unroll
    for (int d = 0; d < 4; ++d) { const unsigned a = w[2 * p][d], b = w[2 * p + 1][d]; w[2 * p][d] = plo(a, b); w[2 * p + 1][d] = phi(a, b); }
#pragma unroll
  for (int p = 0; p < N; ++p) asm volatile("" : "+v"(w[p]));
}
template <int N> __device__ __forceinline__ void dots(const v4u (&w)[16], const unsigned (&xp)[N / 2], float (&a)[8]) {
#pragma unroll
  for (int d = 0; d < 8; ++d) a[d] = 0.f;
#pragma unroll
  for (int p = 0; p < N / 2; ++p)
#pragma unroll
    for (int d = 0; d < 4; ++d) { a[2 * d] = dot2(w[2 * p][d], xp[p], a[2 * d]); a[2 * d + 1] = dot2(w[2 * p + 1][d], xp[p], a[2 * d + 1]); }
}
__device__ __forceinline__ void ln_fin(float sum, float sq, float& m, float& sd) {
  const float mean = sum * (1.f / 1024.f), var = fmaxf(sq * (1.f / 1024.f) - mean * mean, 0.f);
  m = rbf(mean);
  sd = rbf(__builtin_amdgcn_sqrtf(rbf(rbf(var) + 1e-5f)));
}
__device__ __forceinline__ void tile_reduce(LAS unsigned char* L, int dst, const float (&a)[8], int w, int lane) {
  float c0 = tr16(tr32(a[0], a[4]), tr32(a[2], a[6])), c1 = tr16(tr32(a[1], a[5]), tr32(a[3], a[7]));
  c0 += dppf<ROR8>(c0); c1 += dppf<ROR8>(c1);
  *(LAS float*)(L + dst + ((8 * (lane & 7) + 2 * (lane >> 4) + ((lane >> 3) & 1)) * 8 + w) * 4) = (lane & 8) ? c1 : c0;
}
__device__ __forceinline__ void tile_gemv16(LAS unsigned char* L, int dst, const v4u (&wt)[16], int w, int lane, unsigned u) {
  *(LAS unsigned*)(L + L_XV + w * 256 + lane * 4) = u;
  LGKM0();
  const v4u t0 = *(LAS v4u*)(L + L_XV + w * 256 + (lane >> 3) * 32), t1 = *(LAS v4u*)(L + L_XV + w * 256 + (lane >> 3) * 32 + 16);
  const unsigned xp[8] = {t0.x, t0.y, t0.z, t0.w, t1.x, t1.y, t1.z, t1.w};
  float a[8];
  dots<16>(wt, xp, a);
  tile_reduce(L, dst, a, w, lane);
}
__device__ __forceinline__ float sum8(LAS unsigned char* L, int off) {
  const f32x4 t0 = *(LAS f32x4*)(L + off), t1 = *(LAS f32x4*)(L + off + 16);
  return ((t0[0] + t0[1]) + (t0[2] + t0[3])) + ((t1[0] + t1[1]) + (t1[2] + t1[3]));
}
__device__ __forceinline__ v4u slab_reduce(const float (&a)[8]) {
  const float b0 = tr32(a[0], a[4]), b1 = tr32(a[1], a[5]), b2 = tr32(a[2], a[6]), b3 = tr32(a[3], a[7]);
  return v4u{nosent(f2u(tr16(b0, b0))), nosent(f2u(tr16(b1, b1))), nosent(f2u(tr16(b2, b2))), nosent(f2u(tr16(b3, b3)))};
}

__global__ void __launch_bounds__(512, 2)
mk_kernel(const void* p_x, const void* p_k, const void* p_v, const void* p_wq, const void* p_wo, const void* p_w1, const void* p_b1,
          const void* p_w2, const void* p_b2, const void* p_c, const void* p_wm, void* p_out, void* p_ws) {
  __shared__ __attribute__((aligned(16))) unsigned char smem[L_END];
  LAS unsigned char* L = (LAS unsigned char*)smem;
  const int tid = threadIdx.x, lane = tid & 63, w = __builtin_amdgcn_readfirstlane(tid >> 6);
  const int b = blockIdx.x, xg = b & 7, ic = b >> 3, j = xg * 32 + ic;
  const int g4 = lane >> 4, n16 = lane & 15, r8 = lane >> 3, c8 = lane & 7;
  const int kvg = j >> 6, spl = j & 63;
  volatile LAS unsigned* flags = (volatile LAS unsigned*)(L + L_FLAG);
  if (tid < 8) flags[tid] = 0u;
  BAR();
  if (w == 0) STAMP(0);
  const rsrc_t rws = MKR(p_ws, WS_END);
  const bool isA = ic < 8, isQ = ic >= 8 && ic < 16, isR = ic >= 16 && ic < 24, isM = ic >= 24 && ic < 31;
  const bool isX = ic >= 8 && ic < 12;
  const bool isO = ic < 8 || (ic >= 16 && ic < 23) || ic == 31;
  const int ti = xg * 8 + (ic & 7);
  const int mi = xg * 7 + ic - 24;
  const int kbase = j * 262144 + w * 32768;
  const int koff = (4 * ((n16 >> 1) & 1) + (n16 >> 2)) * 256 + g4 * 32 + (n16 & 1) * 16;
  const rsrc_t rk = MKR(p_k, 64 << 20), rv = MKR(p_v, 64 << 20);

  const int stride = isQ ? 8192 : isM ? 7168 : 12288;
  const void* tp = isQ ? p_wq : isM ? p_w1 : p_wm;
  const rsrc_t rt1 = MKR(tp, ic < 31 ? 1024 * stride : 0), rt2 = MKR(tp, (ic >= 8 && ic < 31) ? 1024 * stride : 0);
  const int tvo = r8 * (isA ? 8 : 16) * stride + c8 * 16;
  const int tso = isA ? (512 * (ti & 1) + 64 * w) * stride + (ti >> 1) * 128 : 128 * w * stride + (isQ ? ti * 128 : isM ? mi * 128 : 4096 + ti * 128);
  const rsrc_t rin = MKR(isQ ? p_x : p_c, 2048);
  const unsigned epoch = LD32(rws, 0, WS_EPOCH, AUX_SC1);
#define GET_TAG() unsigned tag = epoch; asm volatile("" : "+v"(tag)); tag = __builtin_amdgcn_readfirstlane(tag) + 0x9E3779B9u
  const unsigned own = LD32(rin, lane * 4, w * 256, 0);
  const v4u xa = LD128(rin, isA ? r8 * 16 : lane * 16, isA ? 1024 * (ti & 1) + 128 * w : 0, 0), xb = LD128(rin, lane * 16, 1024, 0);
  v4u kr[32], vr[16];
#define T_LD(i) { vr[i] = LD128((i) < 8 ? rt1 : rt2, tvo, tso + (i) * stride, WAUX); SCHED(); }
#define K_LD(i) { kr[i] = LD128(rk, koff + ((i) & 1) * 128, kbase + ((i) >> 1) * 2048, AUX_NT); SCHED(); }
#define CAP() { VMWAIT(NF); SCHED(); }
#pragma unroll
  for (int i = 0; i < 8; ++i) T_LD(i)
  {
    const v4u sv = {SENT, SENT, SENT, SENT};
    if (isA) {
      ST128(sv, rws, lane * 16, WS_P2 + ti * 8192 + w * 1024);
      if (w < 4) ST128(sv, rws, lane * 16, WS_P4 + ti * 4096 + w * 1024);
      if (w == 4 && ti < 2 * XREP) ST128(sv, rws, lane * 16, WS_X1 + ti * 1024);
    } else if (isQ) {
      ST128(sv, rws, lane * 16, WS_P1 + ti * 16384 + w * 2048);
      ST128(sv, rws, lane * 16, WS_P1 + ti * 16384 + w * 2048 + 1024);
      if (w == 0 && lane < 16) ST128(sv, rws, lane * 16, WS_ML + ti * 256);
    }
  }
  K_LD(0) K_LD(1)
  if (isA) {
    const unsigned xp[4] = {pk2(silu_bf(blo(xa.x)), silu_bf(bhi(xa.x))), pk2(silu_bf(blo(xa.y)), silu_bf(bhi(xa.y))),
                            pk2(silu_bf(blo(xa.z)), silu_bf(bhi(xa.z))), pk2(silu_bf(blo(xa.w)), silu_bf(bhi(xa.w)))};
    pairup<8>(vr);
    float a[8];
    dots<8>(vr, xp, a);
    tile_reduce(L, L_JP, a, w, lane);
    GET_TAG();
    VMWAIT(0);
    if (lds_arrive(L, F_JOB, lane) == 7u) {
      const float t = sum8(L, L_JP + lane * 32);
      const float tn = dppf<0xF5>(t);
      if (!(lane & 1)) ST128((v4u{f2u(t), tag, f2u(tn), tag}), rws, lane * 8, WS_AP + (ti & 1) * 16384 + (ti >> 1) * 512);
      STAMP(3);
    }
  }
#pragma unroll
  for (int i = 0; i < 8; ++i) { T_LD(8 + i) CAP() K_LD(2 + 2 * i) CAP() K_LD(3 + 2 * i) CAP() }
#pragma unroll
  for (int i = 18; i < KP2 - 2; ++i) { K_LD(i) CAP() }
  v4u s0, s1, c0, c1;
#define H_LOAD(R) { s0 = LD128(R, lane * 16, WS_AP + w * 1024, AUX_SC1); s1 = LD128(R, lane * 16, WS_AP + 16384 + w * 1024, AUX_SC1); \
                    c0 = LD128(R, lane * 16, WS_AP + 8192 + w * 1024, AUX_SC1); c1 = LD128(R, lane * 16, WS_AP + 24576 + w * 1024, AUX_SC1); }
#define H_OK() __all((s0.y == tag) & (s0.w == tag) & (s1.y == tag) & (s1.w == tag) & (c0.y == tag) & (c0.w == tag) & (c1.y == tag) & (c1.w == tag))
  H_LOAD(MKR(p_ws, isQ ? WS_END : 0))
  SCHED();
  K_LD(KP2 - 2) K_LD(KP2 - 1)
  if (isM) {
#pragma unroll
    for (int i = 0; i < 16; ++i) *(LAS v4u*)(L + L_V + w * 16384 + i * 1024 + lane * 16) = vr[i];
  } else if (isQ || isR) {
    unsigned u;
    float n0 = 0.f, n1 = 0.f;
    if (isR) {
      u = pk2(silu_bf(blo(own)), silu_bf(bhi(own)));
    } else {
      float s = 0.f, q = 0.f;
#pragma unroll
      for (int k = 0; k < 4; ++k) {
        const float a0 = blo(xa[k]), a1 = bhi(xa[k]), a2 = blo(xb[k]), a3 = bhi(xb[k]);
        s += (a0 + a1) + (a2 + a3); q += (a0 * a0 + a1 * a1) + (a2 * a2 + a3 * a3);
      }
      float m, sd;
      ln_fin(wavesum(s), wavesum(q), m, sd);
      n0 = rbf(rbf(blo(own) - m) / sd); n1 = rbf(rbf(bhi(own) - m) / sd);
      u = 0u;
    }
    pairup<16>(vr);
    GET_TAG();
    if (isQ) {
      if (w == 0) STAMP(1);
      if (!H_OK()) {
#pragma unroll 1
        for (unsigned sp = 0; sp < SPIN_G; ++sp) {
          CFENCE();
          H_LOAD(rws)
          if (H_OK()) break;
          __builtin_amdgcn_s_sleep(2);
        }
      }
      if (w == 0) STAMP(2);
      u = pk2(rbf(n0 * rbf(1.f + rbf(u2f(c0.x) + u2f(c1.x)))) + rbf(u2f(s0.x) + u2f(s1.x)), rbf(n1 * rbf(1.f + rbf(u2f(c0.z) + u2f(c1.z)))) + rbf(u2f(s0.z) + u2f(s1.z)));
    }
    tile_gemv16(L, L_JP, vr, w, lane, u);
    VMWAIT(0);
    if (lds_arrive(L, F_JOB, lane) == 7u) {
      const float t = sum8(L, L_JP + lane * 32);
      const unsigned p = pk2(t, dppf<0xF5>(t));
      const unsigned pn = dppu<0xAA>(p);
      if (!(lane & 3)) ST128((v4u{p, tag, pn, tag}), rws, lane * 4, isQ ? WS_QB + ti * 256 : WS_MOD + 8192 + ti * 256);
      STAMP(isQ ? 4 : 5);
    }
  }
#pragma unroll
  for (int i = KP2; i < 32; ++i) { K_LD(i) CAP() }
  const int qoff = (c8 * 128 + 16 * g4 + (n16 & 8)) * 4;
  v4u qg[4], vs[16];
#define V_LD(i) { vr[i] = LD128(rv, lane * 16, kbase + (i) * 1024, AUX_NT); SCHED(); }
#define V2_LD(i) { vs[i] = LD128(rv, lane * 16, kbase + 16384 + (i) * 1024, AUX_NT); SCHED(); }
#define Q_LOAD() { _Pragma("unroll") for (int k = 0; k < 4; ++k) qg[k] = LD128(rws, qoff + (k & 1) * 16 + (k >> 1) * 256, WS_QB + kvg * 4096, AUX_SC1); }
#pragma unroll
  for (int i = 0; i < 12; ++i) { V_LD(i) CAP() }
  Q_LOAD()
  SCHED();
  V_LD(12) V_LD(13) V_LD(14) V_LD(15)
  if (w == 7) STAMP(6);
  GET_TAG();
#define Q_OK() __all((qg[0].y == tag) & (qg[0].w == tag) & (qg[1].y == tag) & (qg[1].w == tag) & (qg[2].y == tag) & (qg[2].w == tag) & (qg[3].y == tag) & (qg[3].w == tag))
  if (!Q_OK()) {
    if (lds_arrive(L, F_QLOCK, lane) == 0u) {
#pragma unroll 1
      for (unsigned sp = 0; sp < SPIN_G; ++sp) {
        CFENCE();
        const v2u t = LD64(rws, n16 * 256, WS_QB + kvg * 4096, AUX_SC1);
        if (__all(t.y == tag)) break;
        __builtin_amdgcn_s_sleep(QSLEEP);
      }
      flags[F_QRDY] = 1u;
    } else LDS_WAIT_GE(F_QRDY, 1u, 4);
#pragma unroll 1
    for (unsigned sp = 0; sp < SPIN_G; ++sp) {
      CFENCE();
      Q_LOAD()
      if (Q_OK()) break;
    }
  }
  const v4u qf0 = {qg[0].x, qg[0].z, qg[1].x, qg[1].z}, qf1 = {qg[2].x, qg[2].z, qg[3].x, qg[3].z};
  const bool hf = (lane & 8) != 0;
  float s[16];
#pragma unroll
  for (int t = 0; t < 16; ++t) {
    f32x4 d = {0.f, 0.f, 0.f, 0.f};
    d = mfma(kr[2 * t], qf0, d);
    d = mfma(kr[2 * t + 1], qf1, d);
    const float send = hf ? d[1] : d[2], keep = hf ? d[3] : d[0];
    s[t] = rbf(rbf(keep + dppf<ROR8>(send)) * 0.08838834764831845f);
    if (t & 1) { SCHED(); V2_LD(t >> 1) }
  }
  if (w == 7) STAMP(7);
  if (w == 0) STAMP(8);
  float m = s[0];
#pragma unroll
  for (int t = 1; t < 16; ++t) m = fmaxf(m, s[t]);
  m = fmaxf(m, dppf<ROR8>(m)); m = mx16(m); m = mx32(m);
  SCHED(); V2_LD(8) V2_LD(9)
  float lsum = 0.f;
#pragma unroll
  for (int t = 0; t < 16; ++t) { s[t] = fexp(s[t] - m); lsum += s[t]; }
  SCHED(); V2_LD(10) V2_LD(11)
  lsum += dppf<ROR8>(lsum); lsum = tr16(lsum, lsum); lsum = tr32(lsum, lsum);
  if (lane < 8) { *(LAS float*)(L + L_MX + lane * 32 + w * 4) = m; *(LAS float*)(L + L_LS + lane * 32 + w * 4) = lsum; }
  if (lds_arrive(L, F_SM, lane) == 7u) {
    const float mw = *(LAS float*)(L + L_MX + lane * 4), lw = *(LAS float*)(L + L_LS + lane * 4);
    float M = fmaxf(mw, dppf<0xB1>(mw)); M = fmaxf(M, dppf<0x4E>(M)); M = fmaxf(M, dppf<0x141>(M));
    const float f = fexp(mw - M);
    float l = f * lw; l += dppf<0xB1>(l); l += dppf<0x4E>(l); l += dppf<0x141>(l);
    *(LAS float*)(L + L_FS + lane * 4) = f;
    if (c8 == 0) ST64((v2u{nosent(f2u(M)), nosent(f2u(l))}), rws, r8 * 512, WS_ML + kvg * 4096 + spl * 8);
    STAMP(9);
  }
  unsigned pp2[8];
#pragma unroll
  for (int t = 0; t < 8; ++t) pp2[t] = pk2(s[2 * t], s[2 * t + 1]);
  f32x4 acc[8];
#pragma unroll
  for (int e = 0; e < 8; ++e) acc[e] = f32x4{0.f, 0.f, 0.f, 0.f};
#define PV_GROUP(G, VV) { \
    const v4u pf = {pp2[2 * G], pp2[2 * G + 1], dppu<ROR8>(pp2[2 * G]), dppu<ROR8>(pp2[2 * G + 1])}; \
    _Pragma("unroll") for (int d = 0; d < 4; ++d) { \
      v4u a0, a1; \
      a0.x = plo((VV)[0][d], (VV)[2][d]); a0.y = plo((VV)[4][d], (VV)[6][d]); \
      a0.z = plo((VV)[1][d], (VV)[3][d]); a0.w = plo((VV)[5][d], (VV)[7][d]); \
      a1.x = phi((VV)[0][d], (VV)[2][d]); a1.y = phi((VV)[4][d], (VV)[6][d]); \
      a1.z = phi((VV)[1][d], (VV)[3][d]); a1.w = phi((VV)[5][d], (VV)[7][d]); \
      acc[2 * d] = mfma(a0, pf, acc[2 * d]); \
      acc[2 * d + 1] = mfma(a1, pf, acc[2 * d + 1]); \
    } \
    SCHED(); }
  PV_GROUP(0, vr)
  V2_LD(12) V2_LD(13)
  PV_GROUP(1, vr + 8)
  V2_LD(14) V2_LD(15)
  v4u wa[16], wb[16];
  if (isM) {
#pragma unroll
    for (int i = 0; i < 16; ++i) wa[i] = *(LAS v4u*)(L + L_V + w * 16384 + i * 1024 + lane * 16);
    pairup<16>(wa);
  }
  PV_GROUP(2, vs)
  PV_GROUP(3, vs + 8)
  if (n16 < 8) {
#pragma unroll
    for (int e = 0; e < 8; ++e) *(LAS f32x4*)(L + L_V + w * 16384 + (n16 * 132 + 32 * g4 + 4 * e) * 4) = acc[e];
  }
  if (w == 0) STAMP(10);
  if (w == 7) STAMP(11);
  BAR();
  if (w == 0) STAMP(12);
  if (w < 4) {
    const int h = tid >> 5, pos4 = tid & 31;
    const f32x4 f0 = *(LAS f32x4*)(L + L_FS + h * 32), f1 = *(LAS f32x4*)(L + L_FS + h * 32 + 16);
    f32x4 o = f0[0] * *(LAS f32x4*)(L + L_V + (h * 132 + 4 * pos4) * 4);
#pragma unroll
    for (int ww = 1; ww < 8; ++ww) o += (ww < 4 ? f0[ww & 3] : f1[ww & 3]) * *(LAS f32x4*)(L + L_V + ww * 16384 + (h * 132 + 4 * pos4) * 4);
    ST128((v4u{nosent(f2u(o[0])), nosent(f2u(o[1])), nosent(f2u(o[2])), nosent(f2u(o[3]))}), rws, (tid >> 3) * 8192 + c8 * 16, WS_P1 + kvg * 262144 + spl * 128);
    if (w == 0) STAMP(13);
  }
  const int oi = (xg & 1) * 16 + (ic < 8 ? ic : ic < 23 ? ic - 8 : 15);
  const int xi = xg * 4 + ic - 8;

  if (isO) {
    {
      const rsrc_t rwo = MKR(p_wo, 4096 * 1024 * 2);
#pragma unroll
      for (int i = 0; i < 8; ++i) wb[i] = LD128(rwo, g4 * 4096 + n16 * 16, (kvg * 1024 + 32 * oi + 8 * (i & 3) + (i >> 2)) * 2048 + w * 256, WAUX);
    }
    const int hq = kvg * 8 + (oi >> 2);
    v2u ml; unsigned mo;
#pragma unroll 1
    for (unsigned sp = 0; sp < SPIN_G; ++sp) {
      CFENCE();
      ml = LD64(rws, lane * 8, WS_ML + hq * 512, AUX_SC1);
      mo = LD32(rws, r8 * 8, WS_ML + hq * 512 + w * 64, AUX_SC1);
      if (!__any(umax(umax(ml.x, ml.y), mo) == SENT)) break;
      __builtin_amdgcn_s_sleep(8);
    }
    float M = u2f(ml.x);
    M = mx32(M); M = mx16(M); M = fmaxf(M, dppf<ROR8>(M)); M = fmaxf(M, dppf<ROR4>(M)); M = fmaxf(M, dppf<ROR2>(M)); M = fmaxf(M, dppf<ROR1>(M));
    const float Ls = wavesum(fexp(u2f(ml.x) - M) * u2f(ml.y));
    const float wgt = fexp(u2f(mo) - M) * frcp(Ls);
    pairup<8>(wb);
    if (w == 0) STAMP(14);
    v4u pp;
#pragma unroll 1
    for (unsigned sp = 0; sp < SPIN_G; ++sp) {
      CFENCE();
      pp = LD128(rws, lane * 16, WS_P1 + kvg * 262144 + oi * 8192 + w * 1024, AUX_SC1);
      if (!__any(mx4(pp) == SENT)) break;
    }
    if (w == 0) STAMP(15);
    float o = tr16(tr32(wgt * u2f(pp.x), wgt * u2f(pp.z)), tr32(wgt * u2f(pp.y), wgt * u2f(pp.w)));
    o += dppf<ROR8>(o);
    if (n16 < 8) *(LAS float*)(L + L_PART + ((4 * c8 + g4) * 8 + w) * 4) = o;
    BAR();
    const f32x4 t = *(LAS f32x4*)(L + L_PART + ((8 * g4 + c8) * 8 + (n16 >> 3) * 4) * 4);
    float ov = (t[0] + t[1]) + (t[2] + t[3]);
    ov += dppf<ROR8>(ov);
    const unsigned op = pk2(dppf<0xA0>(ov), dppf<0xF5>(ov));
    const unsigned xp[4] = {bcast16<0>(op), bcast16<2>(op), bcast16<4>(op), bcast16<6>(op)};
    float a[8];
    dots<8>(wb, xp, a);
    const v4u dv = slab_reduce(a);
    if ((g4 & 1) == 0) ST128(dv, rws, (n16 >> 2) * 16384 + (n16 & 3) * 32 + (g4 >> 1) * 16, WS_P2 + w * 65536 + (kvg * 32 + oi) * 128);
    if (w == 0) STAMP(16);
  } else if (isX) {
    float xcol = 0.f, gacol = 0.f, gbcol = 0.f, b2col = 0.f;
    if (w == 0) {
      const int c = lane & 31;
      xcol = blo((unsigned)__builtin_amdgcn_raw_buffer_load_b16(MKR(p_x, 2048), c * 2, xi * 64, 0));
      b2col = blo((unsigned)__builtin_amdgcn_raw_buffer_load_b16(MKR(p_b2, 2048), c * 2, xi * 64, 0));
      v2u ga, gb;
#pragma unroll 1
      for (unsigned sp = 0; sp < SPIN_G; ++sp) {
        CFENCE();
        ga = LD64(rws, (c >> 1) * 8, WS_MOD + 2048 * 4 + xi * 128, AUX_SC1);
        gb = LD64(rws, (c >> 1) * 8, WS_MOD + 5120 * 4 + xi * 128, AUX_SC1);
        if (__all((ga.y == tag) & (gb.y == tag))) break;
        __builtin_amdgcn_s_sleep(1);
      }
      gacol = (c & 1) ? bhi(ga.x) : blo(ga.x);
      gbcol = (c & 1) ? bhi(gb.x) : blo(gb.x);
    }
    v4u p0, p1;
#pragma unroll 1
    for (unsigned sp = 0; sp < SPIN_G; ++sp) {
      CFENCE();
      p0 = LD128(rws, lane * 16, WS_P2 + xi * 16384 + w * 2048, AUX_SC1);
      p1 = LD128(rws, lane * 16, WS_P2 + xi * 16384 + w * 2048 + 1024, AUX_SC1);
      if (!__any(umax(mx4(p0), mx4(p1)) == SENT)) break;
    }
    if (w == 0) STAMP(17);
    float o = tr16(tr32(u2f(p0.x) + u2f(p1.x), u2f(p0.z) + u2f(p1.z)), tr32(u2f(p0.y) + u2f(p1.y), u2f(p0.w) + u2f(p1.w)));
    o += dppf<ROR8>(o);
    if (n16 < 8) *(LAS float*)(L + L_PART + ((4 * c8 + g4) * 8 + w) * 4) = o;
    BAR();
    float x1col = 0.f;
    if (w == 0) {
      const f32x4 t = *(LAS f32x4*)(L + L_PART + ((lane & 31) * 8 + (lane >> 5) * 4) * 4);
      float at = (t[0] + t[1]) + (t[2] + t[3]);
      at = tr32(at, at);
      x1col = rbf(xcol + rbf(gacol * rbf(at)));
      const unsigned xp = nosent((dppu<0xA0>(f2u(x1col)) >> 16) | (dppu<0xF5>(f2u(x1col)) & 0xffff0000u));
#pragma unroll
      for (int r = 0; r < XREP / 4; ++r) ST32(xp, rws, ((lane & 31) >> 1) * 4 + ((lane & 1) + 2 * (lane >> 5)) * 2048, WS_X1 + r * 8192 + xi * 64);
      STAMP(18);
    }
    if (w < 7) {
#pragma unroll 1
      for (unsigned sp = 0; sp < SPIN_G; ++sp) {
        CFENCE();
        p0 = LD128(rws, lane * 16, WS_P4 + xi * 8192 + w * 1024, AUX_SC1);
        if (!__any(mx4(p0) == SENT)) break;
      }
    } else p0 = v4u{0u, 0u, 0u, 0u};
    if (w == 0) STAMP(22);
    o = tr16(tr32(u2f(p0.x), u2f(p0.z)), tr32(u2f(p0.y), u2f(p0.w)));
    o += dppf<ROR8>(o);
    if (n16 < 8) *(LAS float*)(L + L_PART2 + ((4 * c8 + g4) * 8 + w) * 4) = o;
    BAR();
    if (w == 0) {
      const f32x4 t = *(LAS f32x4*)(L + L_PART2 + ((lane & 31) * 8 + (lane >> 5) * 4) * 4);
      float mo = (t[0] + t[1]) + (t[2] + t[3]);
      mo = tr32(mo, mo);
      const float res = x1col + rbf(gbcol * rbf(rbf(mo) + b2col));
      if (lane < 32) ((unsigned short*)p_out)[32 * xi + lane] = (unsigned short)(pk2(res, res) & 0xffffu);
      if (xi == 0 && lane == 0) ST32(tag, rws, 0, WS_EPOCH);
      STAMP(23);
    }
  } else if (isM) {
    {
      const rsrc_t rw2 = MKR(p_w2, 3584 * 1024 * 2);
#pragma unroll
      for (int i = 0; i < 16; ++i) wb[i] = LD128(rw2, g4 * 16 * 2048 + n16 * 16, (64 * mi + i) * 2048 + w * 256, WAUX);
    }
    const float b1v = blo((unsigned)__builtin_amdgcn_raw_buffer_load_b16(MKR(p_b1, 7168), lane * 2, mi * 128, 0));
    v2u sb, cb;
#pragma unroll 1
    for (unsigned sp = 0; sp < SPIN_G; ++sp) {
      CFENCE();
      sb = LD64(rws, lane * 8, WS_MOD + 3072 * 4 + w * 512, AUX_SC1);
      cb = LD64(rws, lane * 8, WS_MOD + 4096 * 4 + w * 512, AUX_SC1);
      if (__all((sb.y == tag) & (cb.y == tag))) break;
      __builtin_amdgcn_s_sleep(1);
    }
    const float c10 = rbf(1.f + blo(cb.x)), c11 = rbf(1.f + bhi(cb.x)), s0 = blo(sb.x), s1 = bhi(sb.x);
    pairup<16>(wb);
    if (w == 0) STAMP(24);
    unsigned xu;
#pragma unroll 1
    for (unsigned sp = 0; sp < SPIN_G; ++sp) {
      CFENCE();
      xu = LD32(rws, lane * 4, WS_X1 + (mi % XREP) * 2048 + w * 256, AUX_SC1);
      if (!__any(xu == SENT)) break;
    }
    if (w == 0) STAMP(19);
    float y0 = blo(xu), y1 = bhi(xu), mu, sd;
#define LN_ROUND(OFF) { \
      float t = tr32(y0 + y1, y0 * y0 + y1 * y1); t = tr16(t, t); t = rowsum16(t); \
      if ((lane & 31) == 0) *(LAS float*)(L + L_RED + OFF + (lane >> 5) * 32 + w * 4) = t; \
      BAR(); \
      ln_fin(sum8(L, L_RED + OFF), sum8(L, L_RED + OFF + 32), mu, sd); \
      const float inv = frcp(sd); \
      y0 = rbf(rbf(y0 - mu) * inv); y1 = rbf(rbf(y1 - mu) * inv); }
    LN_ROUND(0)
    y0 = rbf(rbf(y0 * c10) + s0); y1 = rbf(rbf(y1 * c11) + s1);
    LN_ROUND(64)
    if (w == 0) STAMP(20);
    tile_gemv16(L, L_PART, wa, w, lane, pk2(y0, y1));
    BAR();
    float z = sum8(L, L_PART + lane * 32);
    z = rbf(rbf(z) + b1v);
    const float u = 0.7978845608028654f * (z + 0.044715f * z * z * z);
    const float hv = z * frcp(1.f + fexp(-2.f * u));
    const unsigned hp = pk2(dppf<0xA0>(hv), dppf<0xF5>(hv));
    const unsigned xp[8] = {bcast16<0>(hp), bcast16<2>(hp), bcast16<4>(hp), bcast16<6>(hp), bcast16<8>(hp), bcast16<10>(hp), bcast16<12>(hp), bcast16<14>(hp)};
    float a[8];
    dots<16>(wb, xp, a);
    const v4u dv = slab_reduce(a);
    if ((g4 & 1) == 0) ST128(dv, rws, (n16 >> 2) * 8192 + (n16 & 3) * 32 + (g4 >> 1) * 16, WS_P4 + w * 32768 + mi * 128);
    if (w == 0) STAMP(21);
  }
}

extern "C" void kernel_launch(void* const* d_in, const int* in_sizes, int n_in, void* d_out, int out_size, void* d_ws, size_t ws_size,
                              hipStream_t stream) {
  mk_kernel<<<256, 512, 0, stream>>>(d_in[0], d_in[1], d_in[2], d_in[3], d_in[4], d_in[6], d_in[7], d_in[8], d_in[9], d_in[10], d_in[11], d_out, d_ws);
}
```

```cpp
#include <hip/hip_runtime.h>
#include <stdint.h>

#define LAS __attribute__((address_space(3)))
typedef unsigned v4u __attribute__((ext_vector_type(4)));
typedef unsigned v2u __attribute__((ext_vector_type(2)));
typedef float f32x4 __attribute__((ext_vector_type(4)));
typedef __bf16 bf16x8 __attribute__((ext_vector_type(8)));
typedef __bf16 bf16x2 __attribute__((ext_vector_type(2)));
typedef short s16x4 __attribute__((ext_vector_type(4)));
typedef __amdgpu_buffer_rsrc_t rsrc_t;

#define NF 2
#define NF1 3
#define NF2 6
#define PT0 100
#define PDT 436
#define PDTM 423
#define PBH 80
#define TCAP 5
#define K0 4
#define KP2 14
#define KJ 4
#define KQ 28
#define VQ 8
#define MW 3

constexpr int AUX_NT = 2, AUX_SC1 = 16;
constexpr unsigned SENT = 0xFFFFFFFFu;
constexpr int OOR = 0x40000000;
constexpr int WS_EPOCH = 0;
constexpr int WS_MOD = 4096;
constexpr int WS_QB = 32768;
constexpr int WS_AP = 49152;
constexpr int WS_PROBE = 163840;
constexpr int WS_X1 = 98304;
constexpr int WS_P4 = 262144;
constexpr int WS_P2 = 524288;
constexpr int WS_P1 = 1048576;
constexpr int WS_END = 2097152;
constexpr int L_V = 0;
constexpr int L_XV = 131072;
constexpr int L_Q = L_XV + 4096;
constexpr int L_JP = L_Q + 2048;
constexpr int L_MX = L_JP + 2048;
constexpr int L_LS = L_MX + 256;
constexpr int L_FS = L_LS + 256;
constexpr int L_LSE = L_FS + 256;
constexpr int L_PART = L_LSE + 128;
constexpr int L_PART2 = L_PART + 2048;
constexpr int L_RED = L_PART2 + 1024;
constexpr int L_DUMP = L_RED + 256;
constexpr int L_FLAG = L_DUMP + 256;
constexpr int L_END = L_FLAG + 64;
enum { F_JOB = 1, F_SM = 2, F_QRDY = 3, F_VOTE = 4 };

constexpr unsigned SPIN_G = 400000u, SPIN_L = 8000000u;

__device__ __forceinline__ float u2f(unsigned u) { return __builtin_bit_cast(float, u); }
__device__ __forceinline__ unsigned f2u(float f) { return __builtin_bit_cast(unsigned, f); }
__device__ __forceinline__ float blo(unsigned u) { return u2f(u << 16); }
__device__ __forceinline__ float bhi(unsigned u) { return u2f(u & 0xffff0000u); }
__device__ __forceinline__ unsigned pk2(float a, float b) { bf16x2 v = {(__bf16)a, (__bf16)b}; return __builtin_bit_cast(unsigned, v); }
__device__ __forceinline__ float rbf(float a) { return (float)(__bf16)a; }
__device__ __forceinline__ unsigned plo(unsigned a, unsigned b) { return __builtin_amdgcn_perm(b, a, 0x05040100u); }
__device__ __forceinline__ unsigned phi(unsigned a, unsigned b) { return __builtin_amdgcn_perm(b, a, 0x07060302u); }
__device__ __forceinline__ float dot2(unsigned a, unsigned b, float c) {
  return __builtin_amdgcn_fdot2_f32_bf16(__builtin_bit_cast(bf16x2, a), __builtin_bit_cast(bf16x2, b), c, false);
}
__device__ __forceinline__ f32x4 mfma(v4u a, v4u b, f32x4 c) {
  return __builtin_amdgcn_mfma_f32_16x16x32_bf16(__builtin_bit_cast(bf16x8, a), __builtin_bit_cast(bf16x8, b), c, 0, 0, 0);
}
template <int CTRL> __device__ __forceinline__ unsigned dppu(unsigned x) { return (unsigned)__builtin_amdgcn_mov_dpp((int)x, CTRL, 0xf, 0xf, true); }
template <int CTRL> __device__ __forceinline__ float dppf(float x) { return u2f(dppu<CTRL>(f2u(x))); }
template <int N> __device__ __forceinline__ unsigned bcast16(unsigned x) { return dppu<0x150 + N>(x); }
constexpr int ROR8 = 0x128, ROR4 = 0x124, ROR2 = 0x122, ROR1 = 0x121, QEVEN = 0xA0, QODD = 0xF5;
__device__ __forceinline__ float tr32(float a, float b) { auto s = __builtin_amdgcn_permlane32_swap(f2u(a), f2u(b), false, false); return u2f(s[0]) + u2f(s[1]); }
__device__ __forceinline__ float tr16(float a, float b) { auto s = __builtin_amdgcn_permlane16_swap(f2u(a), f2u(b), false, false); return u2f(s[0]) + u2f(s[1]); }
__device__ __forceinline__ float mx32(float a) { auto s = __builtin_amdgcn_permlane32_swap(f2u(a), f2u(a), false, false); return fmaxf(u2f(s[0]), u2f(s[1])); }
__device__ __forceinline__ float mx16(float a) { auto s = __builtin_amdgcn_permlane16_swap(f2u(a), f2u(a), false, false); return fmaxf(u2f(s[0]), u2f(s[1])); }
__device__ __forceinline__ float rowsum16(float v) { v += dppf<ROR8>(v); v += dppf<ROR4>(v); v += dppf<ROR2>(v); v += dppf<ROR1>(v); return v; }
__device__ __forceinline__ float wavesum(float v) { v = tr32(v, v); v = tr16(v, v); return rowsum16(v); }
__device__ __forceinline__ unsigned nosent(unsigned u) { return u < SENT ? u : SENT - 1u; }
__device__ __forceinline__ float fexp(float x) { return __builtin_amdgcn_exp2f(x * 1.4426950408889634f); }
__device__ __forceinline__ float frcp(float x) { return __builtin_amdgcn_rcpf(x); }
__device__ __forceinline__ float silu_bf(float c) { return rbf(c / (1.f + fexp(-c))); }
__device__ __forceinline__ unsigned umax(unsigned a, unsigned b) { return a > b ? a : b; }
__device__ __forceinline__ unsigned mx4(v4u v) { return umax(umax(v.x, v.y), umax(v.z, v.w)); }

#define LD128(rs, vo, so, aux) __builtin_amdgcn_raw_buffer_load_b128(rs, vo, so, aux)
#define LD64(rs, vo, so, aux) __builtin_amdgcn_raw_buffer_load_b64(rs, vo, so, aux)
#define LD32(rs, vo, so, aux) __builtin_amdgcn_raw_buffer_load_b32(rs, vo, so, aux)
#define LD16(rs, vo, so) (unsigned)__builtin_amdgcn_raw_buffer_load_b16(rs, vo, so, 0)
#define ST128(v, rs, vo, so) __builtin_amdgcn_raw_buffer_store_b128(v, rs, vo, so, AUX_SC1)
#define ST64(v, rs, vo, so) __builtin_amdgcn_raw_buffer_store_b64(v, rs, vo, so, AUX_SC1)
#define ST32(v, rs, vo, so) __builtin_amdgcn_raw_buffer_store_b32(v, rs, vo, so, AUX_SC1)
#define CFENCE() asm volatile("" ::: "memory")
#define SCHED() __builtin_amdgcn_sched_barrier(0)
#define BAR() asm volatile("s_waitcnt lgkmcnt(0)\n\ts_barrier" ::: "memory")
#define LGKM0() asm volatile("s_waitcnt lgkmcnt(0)" ::: "memory")
#define VMWAIT(n) asm volatile("s_waitcnt vmcnt(%0)" ::"n"(n) : "memory")
#define MKR(p, n) __builtin_amdgcn_make_buffer_rsrc((void*)(p), 0, (n), 0x00020000)
#define PINV(x) asm volatile("" : "+v"(x))
#define PINS(x) asm volatile("" : "+s"(x))
#define LDSP(T, off) ((LAS T*)(size_t)(unsigned)(off))
#define ZF4 f32x4{0.f, 0.f, 0.f, 0.f}

__device__ __forceinline__ unsigned lds_arrive(LAS unsigned char* L, int idx, int lane) {
  LGKM0();
  unsigned old = 0;
  if (lane == 0) old = __hip_atomic_fetch_add((LAS unsigned*)(L + L_FLAG) + idx, 1u, __ATOMIC_RELAXED, __HIP_MEMORY_SCOPE_WORKGROUP);
  old = __builtin_amdgcn_readfirstlane(old);
  CFENCE();
  return old;
}
template <int N> __device__ __forceinline__ void pairup(v4u (&w)[16]) {
#pragma unroll
  for (int p = 0; p < N / 2; ++p)
#pragma unroll
    for (int d = 0; d < 4; ++d) { const unsigned a = w[2 * p][d], b = w[2 * p + 1][d]; w[2 * p][d] = plo(a, b); w[2 * p + 1][d] = phi(a, b); }
#pragma unroll
  for (int p = 0; p < N; ++p) PINV(w[p]);
}
template <int N> __device__ __forceinline__ void dots(const v4u (&w)[16], const unsigned (&xp)[N / 2], float (&a)[8]) {
#pragma unroll
  for (int d = 0; d < 8; ++d) a[d] = 0.f;
#pragma unroll
  for (int p = 0; p < N / 2; ++p)
#pragma unroll
    for (int d = 0; d < 4; ++d) { a[2 * d] = dot2(w[2 * p][d], xp[p], a[2 * d]); a[2 * d + 1] = dot2(w[2 * p + 1][d], xp[p], a[2 * d + 1]); }
}
template <int N> __device__ __forceinline__ void mkfrag(const v4u (&w)[16], v4u (&f)[16]) {
#pragma unroll
  for (int h = 0; h < N / 8; ++h) {
#pragma unroll
    for (int e = 0; e < 8; ++e)
#pragma unroll
      for (int p = 0; p < 4; ++p) { const unsigned a = w[8 * h + 2 * p][e >> 1], b = w[8 * h + 2 * p + 1][e >> 1]; f[e * (N / 8) + h][p] = (e & 1) ? phi(a, b) : plo(a, b); }
#pragma unroll
    for (int e = 0; e < 8; ++e) PINV(f[e * (N / 8) + h]);
  }
}
__device__ __forceinline__ void ln_fin(float piv, float sum, float sq, float& m, float& sd) {
  const float mean = sum * (1.f / 1024.f), var = fmaxf(sq * (1.f / 1024.f) - mean * mean, 0.f);
  m = rbf(piv + mean);
  sd = rbf(__builtin_amdgcn_sqrtf(rbf(rbf(var) + 1e-5f)));
}
__device__ __forceinline__ void tile_reduce(LAS unsigned char* L, const float (&a)[8], int w, int lane) {
  float c0 = tr16(tr32(a[0], a[4]), tr32(a[2], a[6])), c1 = tr16(tr32(a[1], a[5]), tr32(a[3], a[7]));
  c0 += dppf<ROR8>(c0); c1 += dppf<ROR8>(c1);
  *(LAS float*)(L + L_JP + ((8 * (lane & 7) + 2 * (lane >> 4) + ((lane >> 3) & 1)) * 8 + w) * 4) = (lane & 8) ? c1 : c0;
}
__device__ __forceinline__ float sum4(f32x4 t) { return (t[0] + t[1]) + (t[2] + t[3]); }
__device__ __forceinline__ float sum8(LAS unsigned char* L, int off) { return sum4(*(LAS f32x4*)(L + off)) + sum4(*(LAS f32x4*)(L + off + 16)); }
__device__ __forceinline__ v4u tagbytes(unsigned t) { return v4u{t & 255u, (t >> 8) & 255u, (t >> 16) & 255u, t >> 24}; }
__device__ __forceinline__ v4u lowtag(f32x4 v, v4u tb) {
  return v4u{(f2u(v[0]) & 0xFFFFFF00u) | tb.x, (f2u(v[1]) & 0xFFFFFF00u) | tb.y, (f2u(v[2]) & 0xFFFFFF00u) | tb.z, (f2u(v[3]) & 0xFFFFFF00u) | tb.w};
}
__device__ __forceinline__ unsigned lowbytes(v4u p) {
  return __builtin_amdgcn_perm(__builtin_amdgcn_perm(p.w, p.z, 0x04000400u), __builtin_amdgcn_perm(p.y, p.x, 0x04000400u), 0x05040100u);
}
__device__ __forceinline__ float val(unsigned u) { return u2f(u & 0xFFFFFF00u); }
__device__ __forceinline__ float linesum(float x, float y, float z, float w_) { float o = tr16(tr32(x, z), tr32(y, w_)); return o + dppf<ROR8>(o); }
__device__ __forceinline__ f32x4 pick4(f32x4 (&d)[8], bool sel) {
#pragma unroll
  for (int e = 0; e < 8; ++e) PINV(d[e]);
  return f32x4{sel ? d[4][0] : d[0][0], sel ? d[5][0] : d[1][0], sel ? d[6][0] : d[2][0], sel ? d[7][0] : d[3][0]};
}

__global__ void __launch_bounds__(512, 2)
mk_kernel(const void* p_x, const void* p_k, const void* p_v, const void* p_wq, const void* p_wo, const void* p_w1, const void* p_b1,
          const void* p_w2, const void* p_b2, const void* p_c, const void* p_wm, void* p_out, void* p_ws) {
  __shared__ __attribute__((aligned(16))) unsigned char smem[L_END];
  LAS unsigned char* L = (LAS unsigned char*)smem;
  const int LB = (int)(unsigned)(size_t)L;
  const int tid = threadIdx.x, lane = tid & 63, w = __builtin_amdgcn_readfirstlane(tid >> 6);
  const int b = blockIdx.x, xg = b & 7, ic = b >> 3, j = xg * 32 + ic;
  const int g4 = lane >> 4, n16 = lane & 15, r8 = lane >> 3, c8 = lane & 7;
  const int kvg = j >> 6, spl = j & 63;
  volatile LAS unsigned* flags = (volatile LAS unsigned*)(L + L_FLAG);
  const unsigned t_start = (unsigned)__builtin_amdgcn_s_memrealtime();
  flags[tid & 7] = 0u;
  asm volatile("" :: "s"(p_k), "s"(p_ws), "s"(p_wm), "s"(p_c), "s"(p_out), "s"(p_b2));
  const int kbase = j * 262144 + w * 32768 + w * 4096 + ((ic >> 2) & 1) * 2048;
  const int koff = (4 * ((n16 >> 1) & 1) + (n16 >> 2)) * 256 + g4 * 32 + (n16 & 1) * 16;
  const rsrc_t rk = MKR(p_k, 64 << 20), rv = MKR(p_v, 64 << 20);
  v4u kr[32], vr[16];
#define KI(i) ((i) < K0 ? (i) + 8 : (i) < K0 + 8 ? (i) - K0 : (i))
#define K_LD(i) { kr[KI(i)] = LD128(KI(i) >= 8 ? rk : rk4, koff + (KI(i) & 1) * 128, kbase ^ ((KI(i) >> 1) * 2048), AUX_NT); SCHED(); }
  { const rsrc_t rk4 = rk;
#pragma unroll
    for (int i = 0; i < K0; ++i) K_LD(i) }
  const rsrc_t rws = MKR(p_ws, WS_END);
  const bool isA = ic < 8, isQ = ic >= 8 && ic < 16, isR = ic >= 16 && ic < 24, isM = ic >= 24 && ic < 31;
  const bool isX = (ic >> 2) == 3;
  const bool isO = ((0x80F700FFu >> ic) & 1u) != 0u;
  const int ti = xg * 8 + (ic & 7);
  const int mi = xg * 7 + ic - 24;
  const int oi = (xg & 1) * 16 + (ic == 31 ? 11 : (ic & 7) + ((ic >> 1) & 8));
  const int xi = xg * 4 + ic - 12;
  const int mic = 24 + ((xg + 3) & 7);
  const bool donor = ((((1u << 11) | (1u << 19) | ((1u << mic) & 0x7FFFFFFFu)) >> ic) & 1u) != 0u;
  const int rs = ic + (6 + 2 * (xg & 1)) * w;
  const bool recv = ic == 31 || (ic < 6 + 2 * (xg & 1) && rs < (xg == 4 ? 8 : 16));
  const int dw = ic == 31 ? w : rs & 7, dic = ic == 31 ? 11 : rs < 8 ? 19 : mic;
  const int xbase = (xg * 32 + dic) * 262144 + dw * 32768 + (dw >> 1) * 8192;
  const rsrc_t rk4 = MKR(p_k, donor ? 0 : 64 << 20), rv4 = MKR(p_v, donor ? 0 : 64 << 20);
  int fast = 3;
#define UPD(s) { const int lag_ = (int)((unsigned)__builtin_amdgcn_s_memrealtime() - t_start) - (PT0 + (isM ? (s) * PDTM / 16 : (s) * PDT / 16)); \
    fast = lag_ > PBH ? 7 : lag_ > 0 ? 5 : 3; SCHED(); }
#define CAP() { asm volatile("s_waitcnt vmcnt(%1)\n\ts_cmp_eq_u32 %0, 7\n\ts_cbranch_scc1 1f\n\ts_waitcnt vmcnt(%2)\n\ts_cmp_eq_u32 %0, 5\n\ts_cbranch_scc1 1f\n\ts_waitcnt vmcnt(%3)\n1:" \
    :: "s"(fast), "n"(NF2), "n"(NF1), "n"(NF) : "scc", "memory"); SCHED(); }

  const bool hasT = ic >= 8 && ic < 31;
  const int stride = hasT ? (int)((0x1C00300020000400ull >> (16 * (ic >> 3))) & 0xFFFFull) : 1024;
  const void* tq = isQ ? p_wq : p_wm;
  const void* tp = isM ? p_w1 : tq;
  const int tszr = recv ? 64 << 20 : 0, tsz = hasT ? stride << 10 : tszr;
  const rsrc_t rt1 = MKR(hasT ? tp : p_k, tsz), rt2 = MKR(hasT ? tp : p_v, tsz);
  const int tvo = r8 * (hasT ? stride << 4 : 128) + c8 * 16;
  const int tsoT = ((w * stride + (isM ? mi : ti)) << 7) + (isR ? 4096 : 0), tsoX = recv ? xbase : 0;
  const int tso = hasT ? tsoT : tsoX;
  const int tso2 = tso + (hasT ? stride << 3 : 0);
  const rsrc_t rin = MKR(isQ ? p_x : p_c, 2048);
  const unsigned epoch = LD32(rws, 0, WS_EPOCH, AUX_SC1);
#define GET_TAG() unsigned tag = epoch; PINV(tag); tag = __builtin_amdgcn_readfirstlane(tag) + 0x9E3779B9u
  const unsigned own = LD32(rin, lane * 4, w * 256, 0);
  const int xao = 1024 * (ic & 1) + 128 * w;
  const v4u xa = LD128(rin, (isA ? r8 : lane) * 16, isA ? xao : 0, 0), xb = LD128(rin, lane * 16, 1024, 0);
  SCHED();
  BAR();
#define T_LD(i) { vr[i] = (i) < 8 ? LD128(rt1, tvo, tso + (i) * stride, AUX_NT) : LD128(rt2, tvo, tso2 + ((i) - 8) * stride, AUX_NT); SCHED(); }
  v4u va[16];
  if (isA) {
    if (w == 4 && (ti & 7) < 5) ST128((v4u{SENT, SENT, SENT, SENT}), rws, lane * 16, WS_X1 + ti * 1024);
    ST32(0u, rws, (w < 6 && lane < 2) ? lane * 8192 : OOR, WS_PROBE + w * 16384 + ti * 128);
    const rsrc_t rta = MKR(p_wm, 1024 * 12288);
#pragma unroll
    for (int i = 0; i < 8; ++i) { va[i] = LD128(rta, r8 * 8 * 12288 + c8 * 16, (512 * (ti & 1) + 64 * w + i) * 12288 + (ti >> 1) * 128, AUX_NT); SCHED(); }
  }
  K_LD(K0) K_LD(K0 + 1)
  if (isA) {
    if (w < 6) {
      const int first = w & 1, line = WS_PROBE + w * 16384 + ti * 128;
      unsigned long long t0, t1, t2; unsigned tmp;
      asm volatile("s_load_dword %3, %4, %7\n\ts_waitcnt vmcnt(10) lgkmcnt(0)\n\t"
                   "s_memtime %0\n\ts_load_dword %3, %4, %5\n\ts_waitcnt lgkmcnt(0)\n\t"
                   "s_memtime %1\n\ts_load_dword %3, %4, %6\n\ts_waitcnt lgkmcnt(0)\n\t"
                   "s_memtime %2\n\ts_waitcnt lgkmcnt(0)"
                   : "=&s"(t0), "=&s"(t1), "=&s"(t2), "=&s"(tmp)
                   : "s"(p_ws), "s"(line + first * 8192), "s"(line + (first ^ 1) * 8192), "s"(WS_EPOCH + 128) : "memory");
      const bool second_faster = (unsigned)t2 - (unsigned)t1 < (unsigned)t1 - (unsigned)t0;
      const unsigned vote = (unsigned)((second_faster ? first ^ 1 : first) ^ (xg >> 2));
      if (lane == 0) __hip_atomic_fetch_add((LAS unsigned*)(L + L_FLAG) + F_VOTE, vote, __ATOMIC_RELAXED, __HIP_MEMORY_SCOPE_WORKGROUP);
    }
    const unsigned xp[4] = {pk2(silu_bf(blo(xa.x)), silu_bf(bhi(xa.x))), pk2(silu_bf(blo(xa.y)), silu_bf(bhi(xa.y))),
                            pk2(silu_bf(blo(xa.z)), silu_bf(bhi(xa.z))), pk2(silu_bf(blo(xa.w)), silu_bf(bhi(xa.w)))};
    pairup<8>(va);
    float a[8];
    dots<8>(va, xp, a);
    tile_reduce(L, a, w, lane);
    GET_TAG();
    if (w == 4) VMWAIT(0);
    if (lds_arrive(L, F_JOB, lane) == 7u) {
      const unsigned t = f2u(sum8(L, L_JP + lane * 32)), tg = tag ^ flags[F_VOTE];
      const unsigned tn = dppu<QODD>(t);
      if (!(lane & 1)) ST128((v4u{t, tg, tn, tg}), rws, lane * 8, WS_AP + (ti & 1) * 16384 + (ti >> 1) * 512);
    }
  }
#pragma unroll
  for (int i = 0; i < 16; ++i) {
    T_LD(i)
    if (i >= TCAP) { VMWAIT(TCAP); SCHED(); }
  }
#pragma unroll
  for (int i = K0 + 2; i < KP2 - KJ; ++i) { if (i == K0 + 2) UPD(16 + i) K_LD(i) CAP() }
  v4u s0, s1, c0, c1;
#define H_LOAD(R) { s0 = LD128(R, lane * 16, WS_AP + w * 1024, AUX_SC1); s1 = LD128(R, lane * 16, WS_AP + 16384 + w * 1024, AUX_SC1); \
                    c0 = LD128(R, lane * 16, WS_AP + 8192 + w * 1024, AUX_SC1); c1 = LD128(R, lane * 16, WS_AP + 24576 + w * 1024, AUX_SC1); }
#define H_OK() __all((((s0.y ^ tag) | (s0.w ^ tag) | (s1.y ^ tag) | (s1.w ^ tag) | (c0.y ^ tag) | (c0.w ^ tag) | (c1.y ^ tag) | (c1.w ^ tag)) & ~7u) == 0u)
  H_LOAD(MKR(p_ws, isQ ? WS_END : 0))
  SCHED();
#pragma unroll
  for (int i = KP2 - KJ; i < KP2; ++i) K_LD(i)
  float xpiv = 0.f;
  if (isM || recv) {
#pragma unroll
    for (int i = 0; i < 16; ++i) *(LAS v4u*)(L + L_V + w * 16384 + i * 1024 + lane * 16) = vr[i];
  } else if (isQ || isR) {
    unsigned u;
    float n0 = 0.f, n1 = 0.f;
    if (isR) {
      u = pk2(silu_bf(blo(own)), silu_bf(bhi(own)));
    } else {
      float s = 0.f, q = 0.f;
#pragma unroll
      for (int k = 0; k < 4; ++k) s += (blo(xa[k]) + bhi(xa[k])) + (blo(xb[k]) + bhi(xb[k]));
      const float mean = wavesum(s) * (1.f / 1024.f);
#pragma unroll
      for (int k = 0; k < 4; ++k) {
        const float a0 = blo(xa[k]) - mean, a1 = bhi(xa[k]) - mean, a2 = blo(xb[k]) - mean, a3 = bhi(xb[k]) - mean;
        q += (a0 * a0 + a1 * a1) + (a2 * a2 + a3 * a3);
      }
      float m, sd;
      ln_fin(mean, 0.f, wavesum(q), m, sd);
      xpiv = m;
      n0 = rbf(rbf(blo(own) - m) / sd); n1 = rbf(rbf(bhi(own) - m) / sd);
      u = 0u;
    }
    pairup<16>(vr);
    GET_TAG();
    if (isQ) {
      if (!H_OK()) {
#pragma unroll 1
        for (unsigned sp = 0; sp < SPIN_G; ++sp) {
          CFENCE();
          H_LOAD(rws)
          if (H_OK()) break;
          __builtin_amdgcn_s_sleep(2);
        }
      }
      const unsigned vs = ((s0.y ^ tag) & 7u) + ((s1.y ^ tag) & 7u) + ((c0.y ^ tag) & 7u) + ((c1.y ^ tag) & 7u);
      if ((lane & 31) == 0) __hip_atomic_fetch_add((LAS unsigned*)(L + L_FLAG) + F_VOTE + (w >> 1), vs, __ATOMIC_RELAXED, __HIP_MEMORY_SCOPE_WORKGROUP);
      u = pk2(rbf(n0 * rbf(1.f + rbf(u2f(c0.x) + u2f(c1.x)))) + rbf(u2f(s0.x) + u2f(s1.x)), rbf(n1 * rbf(1.f + rbf(u2f(c0.z) + u2f(c1.z)))) + rbf(u2f(s0.z) + u2f(s1.z)));
    }
    *(LAS unsigned*)(L + L_XV + w * 512 + lane * 4) = u;
    LGKM0();
    const v4u t0 = *(LAS v4u*)(L + L_XV + w * 512 + r8 * 32), t1 = *(LAS v4u*)(L + L_XV + w * 512 + r8 * 32 + 16);
    const unsigned xp[8] = {t0.x, t0.y, t0.z, t0.w, t1.x, t1.y, t1.z, t1.w};
    float a[8];
    dots<16>(vr, xp, a);
    tile_reduce(L, a, w, lane);
    if (lds_arrive(L, F_JOB, lane) == 7u) {
      const float t = sum8(L, L_JP + lane * 32);
      const unsigned p = pk2(t, dppf<QODD>(t));
      const unsigned pn = dppu<0xAA>(p);
      unsigned tg = tag;
      if (isQ) tg ^= (flags[F_VOTE] > 48u ? 1u : 0u) | (flags[F_VOTE + 1] > 48u ? 2u : 0u) | (flags[F_VOTE + 2] > 48u ? 4u : 0u) | (flags[F_VOTE + 3] > 48u ? 8u : 0u);
      if (!(lane & 3)) ST128((v4u{p, tg, pn, tg}), rws, lane * 4, isQ ? WS_QB + ti * 256 : WS_MOD + 8192 + ti * 256);
    }
  }
#pragma unroll
  for (int i = KP2; i < KQ; ++i) { if (((i - KP2) & 1) == 0) UPD(16 + i) K_LD(i) CAP() }
  v4u qg[4], vs[16];
#define Q_LOAD() { _Pragma("unroll") for (int k = 0; k < 4; ++k) qg[k] = LD128(rws, lane * 16, WS_QB + kvg * 4096 + k * 1024, AUX_SC1); SCHED(); }
  if (w == 0) Q_LOAD()
#pragma unroll
  for (int i = KQ; i < 32; ++i) { K_LD(i) CAP() }
  GET_TAG();
  if (w == 0) {
#define Q_OK() __all((((qg[0].y ^ tag) | (qg[0].w ^ tag) | (qg[1].y ^ tag) | (qg[1].w ^ tag) | (qg[2].y ^ tag) | (qg[2].w ^ tag) | (qg[3].y ^ tag) | (qg[3].w ^ tag)) & ~15u) == 0u)
    if (!Q_OK()) {
#pragma unroll 1
      for (unsigned sp = 0; sp < SPIN_G; ++sp) {
        __builtin_amdgcn_s_sleep(16);
        CFENCE();
        Q_LOAD()
        if (Q_OK()) break;
      }
    }
#pragma unroll
    for (int k = 0; k < 4; ++k) *(LAS v2u*)(L + L_Q + k * 512 + lane * 8) = v2u{qg[k].x, qg[k].z};
    LGKM0();
    flags[F_QRDY] = 16u + ((qg[0].y ^ tag) & 15u);
  }
#define V_LD(i) { vr[i] = LD128((i) < 8 ? rv4 : rv, lane * 16, kbase ^ ((i) * 1024), AUX_NT); SCHED(); }
#define V2_LD(i) { vs[i] = LD128(rv, lane * 16, kbase ^ (16384 + (i) * 1024), AUX_NT); SCHED(); }
#define VV(i) { if ((i) < 16) V_LD((i) & 15) else V2_LD((i) & 15) }
#define CV(i) { if (((i) & 1) == 0) UPD(48 + (i)) CAP() VV(i) }
#pragma unroll
  for (int i = 0; i < VQ; ++i) CV(i)
  for (unsigned sp = 0; flags[F_QRDY] == 0u && sp < SPIN_L; ++sp) __builtin_amdgcn_s_sleep(2);
  CFENCE();
  const unsigned hmask = __builtin_amdgcn_readfirstlane(flags[F_QRDY]);
#define HOME(g) (int)((((hmask >> ((g) & 3)) ^ ((g) >> 2)) & 1u) * 8192u)
  const v4u qf0 = *(LAS v4u*)(L + L_Q + c8 * 256 + g4 * 32 + (n16 & 8) * 2), qf1 = *(LAS v4u*)(L + L_Q + c8 * 256 + 128 + g4 * 32 + (n16 & 8) * 2);
  const bool hf = (lane & 8) != 0;
  float s[20];
#define QK_TILE(S, K0_, K1_) { f32x4 d = mfma(K0_, qf0, ZF4); d = mfma(K1_, qf1, d); \
    const float send = hf ? d[1] : d[2], keep = hf ? d[3] : d[0]; S = rbf(rbf(keep + dppf<ROR8>(send)) * 0.08838834764831845f); }
#pragma unroll
  for (int t = 0; t < 16; ++t) {
    QK_TILE(s[t], kr[2 * t], kr[2 * t + 1])
    if (t & 1) { SCHED(); CV(VQ + (t >> 1)) }
  }
#pragma unroll
  for (int t = 0; t < 4; ++t) s[t] = donor ? -INFINITY : s[t];
#pragma unroll
  for (int t = 16; t < 20; ++t) s[t] = -INFINITY;
  if (recv) {
#pragma unroll
    for (int t = 0; t < 4; ++t) {
      const v4u k0 = *(LAS v4u*)(L + L_V + w * 16384 + t * 2048 + koff), k1 = *(LAS v4u*)(L + L_V + w * 16384 + t * 2048 + koff + 128);
      QK_TILE(s[16 + t], k0, k1)
    }
  }
  float m = s[0];
#pragma unroll
  for (int t = 1; t < 20; ++t) m = fmaxf(m, s[t]);
  m = fmaxf(m, dppf<ROR8>(m)); m = mx16(m); m = mx32(m);
  SCHED(); CV(VQ + 8) CV(VQ + 9)
  float lsum = 0.f;
#pragma unroll
  for (int t = 0; t < 20; ++t) { s[t] = fexp(s[t] - m); lsum += s[t]; }
  SCHED(); CV(VQ + 10) CV(VQ + 11)
  lsum += dppf<ROR8>(lsum); lsum = tr16(lsum, lsum); lsum = tr32(lsum, lsum);
  if (lane < 8) { *(LAS float*)(L + L_MX + lane * 32 + w * 4) = m; *(LAS float*)(L + L_LS + lane * 32 + w * 4) = lsum; }
  if (lds_arrive(L, F_SM, lane) == 7u) {
    const float mw = *(LAS float*)(L + L_MX + lane * 4), lw = *(LAS float*)(L + L_LS + lane * 4);
    float M = fmaxf(mw, dppf<0xB1>(mw)); M = fmaxf(M, dppf<0x4E>(M)); M = fmaxf(M, dppf<0x141>(M));
    const float f = fexp(mw - M);
    float l = f * lw; l += dppf<0xB1>(l); l += dppf<0x4E>(l); l += dppf<0x141>(l);
    *(LAS float*)(L + L_FS + lane * 4) = f * frcp(l);
    if (c8 == 0) *(LAS float*)(L + L_LSE + r8 * 4) = fmaf(M, 1.4426950408889634f, __builtin_amdgcn_logf(l));
  }
  unsigned pp2[10];
#pragma unroll
  for (int t = 0; t < 10; ++t) pp2[t] = pk2(s[2 * t], s[2 * t + 1]);
  f32x4 acc[4];
#pragma unroll
  for (int e = 0; e < 4; ++e) acc[e] = ZF4;
#define P_FRAGS(G) \
    const unsigned q0_ = dppu<ROR8>(pp2[2 * G]), q1_ = dppu<ROR8>(pp2[2 * G + 1]); \
    v4u pl = {hf ? 0u : pp2[2 * G], hf ? 0u : pp2[2 * G + 1], hf ? 0u : q0_, hf ? 0u : q1_}; \
    v4u ph = {hf ? q0_ : 0u, hf ? q1_ : 0u, hf ? pp2[2 * G] : 0u, hf ? pp2[2 * G + 1] : 0u};
#define PV_BODY(VV_) { \
    _Pragma("unroll") for (int d = 0; d < 4; ++d) { \
      v4u a0, a1; \
      a0.x = plo((VV_)[0][d], (VV_)[2][d]); a0.y = plo((VV_)[4][d], (VV_)[6][d]); \
      a0.z = plo((VV_)[1][d], (VV_)[3][d]); a0.w = plo((VV_)[5][d], (VV_)[7][d]); \
      a1.x = phi((VV_)[0][d], (VV_)[2][d]); a1.y = phi((VV_)[4][d], (VV_)[6][d]); \
      a1.z = phi((VV_)[1][d], (VV_)[3][d]); a1.w = phi((VV_)[5][d], (VV_)[7][d]); \
      acc[2 * (d & 1)] = mfma(a0, d < 2 ? pl : ph, acc[2 * (d & 1)]); \
      acc[2 * (d & 1) + 1] = mfma(a1, d < 2 ? pl : ph, acc[2 * (d & 1) + 1]); \
    } \
    SCHED(); }
#define PV_GROUP(G, VV_) { P_FRAGS(G) PV_BODY(VV_) }
  CV(VQ + 12) CV(VQ + 13)
  PV_GROUP(0, vr)
  CV(VQ + 14) CV(VQ + 15)
  PV_GROUP(1, vr + 8)
  v2u e0, e1, e2, e3; unsigned e4, e5;
#define M_LD() { e0 = LD64(rws, lane * 8, WS_MOD + 3072 * 4 + w * 512, AUX_SC1); e1 = LD64(rws, lane * 8, WS_MOD + 4096 * 4 + w * 512, AUX_SC1); }
#define X_LD() { e0 = LD64(rws, (lane & 30) * 4, WS_MOD + 2048 * 4 + xi * 128, AUX_SC1); e1 = LD64(rws, (lane & 30) * 4, WS_MOD + 3072 * 4 + xi * 128, AUX_SC1); \
                 e2 = LD64(rws, (lane & 30) * 4, WS_MOD + 4096 * 4 + xi * 128, AUX_SC1); e3 = LD64(rws, (lane & 30) * 4, WS_MOD + 5120 * 4 + xi * 128, AUX_SC1); }
  if (isM) {
    M_LD()
    e4 = LD16(MKR(p_b1, 7168), lane * 2, mi * 128);
  } else if (isX && w == 0) {
    X_LD()
    e4 = LD16(MKR(p_x, 2048), (lane & 31) * 2, xi * 64);
    e5 = LD16(MKR(p_b2, 2048), (lane & 31) * 2, xi * 64);
  }
  SCHED();
  VV(VQ + 16) VV(VQ + 17)
  v4u wa[16], wb[16];
  if (isM) {
#pragma unroll
    for (int i = 0; i < 16; ++i) {
      const int ad = L_V + w * 16384 + (8 * (g4 & 1) + (n16 >> 2)) * 1024 + (g4 >> 1) * 128 + (n16 & 3) * 8 + (i >> 2) * 256 + (i & 3) * 32;
      const v2u t0 = __builtin_bit_cast(v2u, __builtin_amdgcn_ds_read_tr16_b64_v4i16((LAS s16x4*)(L + ad)));
      const v2u t1 = __builtin_bit_cast(v2u, __builtin_amdgcn_ds_read_tr16_b64_v4i16((LAS s16x4*)(L + ad + 4096)));
      wa[i] = v4u{t0.x, t0.y, t1.x, t1.y};
    }
    if (!__all((e0.y == tag) & (e1.y == tag))) {
#pragma unroll 1
      for (unsigned sp = 0; sp < SPIN_G; ++sp) {
        CFENCE();
        M_LD()
        if (__all((e0.y == tag) & (e1.y == tag))) break;
        __builtin_amdgcn_s_sleep(8);
      }
    }
    const float ca = rbf(1.f + blo(e1.x)), cb = rbf(1.f + bhi(e1.x)), sa = blo(e0.x), sb = bhi(e0.x);
    *(LAS unsigned*)(L + L_XV + w * 512 + lane * 4) = pk2(ca, cb);
    *(LAS unsigned*)(L + L_XV + w * 512 + 256 + lane * 4) = e0.x;
    LGKM0();
    f32x4 d[4] = {ZF4, ZF4, ZF4, ZF4};
#pragma unroll
    for (int k = 0; k < 4; ++k) {
      v4u a = *(LAS v4u*)(L + L_XV + w * 512 + (n16 & 1) * 256 + k * 64 + g4 * 16);
      if (n16 & 2) a = v4u{0x3F803F80u, 0x3F803F80u, 0x3F803F80u, 0x3F803F80u};
#pragma unroll
      for (int c = 0; c < 4; ++c) d[c] = mfma(a, wa[4 * k + c], d[c]);
    }
    *(LAS f32x4*)(L + L_V + w * 16384 + 8192 + lane * 16) = g4 == 0 ? d[0] : g4 == 1 ? d[1] : g4 == 2 ? d[2] : d[3];
    const float q0 = wavesum(ca + cb), q1 = wavesum(ca * ca + cb * cb), q2 = wavesum(sa + sb), q3 = wavesum(sa * sa + sb * sb), q4 = wavesum(ca * sa + cb * sb);
    if (lane < 5) *(LAS float*)(L + L_RED + lane * 32 + w * 4) = lane == 0 ? q0 : lane == 1 ? q1 : lane == 2 ? q2 : lane == 3 ? q3 : q4;
  } else if (recv) {
#pragma unroll
    for (int i = 0; i < 8; ++i) wb[i] = *(LAS v4u*)(L + L_V + w * 16384 + 8192 + i * 1024 + lane * 16);
    PV_GROUP(4, wb)
  }
#pragma unroll
  for (int i = VQ + 18; i < 32; ++i) CV(i)
  PV_GROUP(2, vs)
  int a_pw = LB + L_V + w * 16384 + (c8 * 132 + 32 * g4 + 2 * (n16 & 8)) * 4;
  P_FRAGS(3)
  PINV(pl); PINV(ph);
  int a_mg = LB + L_V + ((tid >> 5) * 132 + 4 * (tid & 31)) * 4, a_mg2 = a_mg + 65536, a_fs = LB + L_FS + (tid >> 5) * 32, a_ls = LB + L_LSE + (tid >> 5) * 4;
  int v_p1 = (tid >> 3) * 16384 + c8 * 16, s_p1 = WS_P1 + (kvg & 1) * 524288 + spl * 128 + HOME(2 * kvg + ((w >> 1) & 1));
  int v_wo = g4 * 4096 + n16 * 16, s_wo = (kvg * 1024 + 32 * oi) * 2048 + w * 256, nsh = 4 * (lane & 1);
  PINV(a_pw); PINV(a_mg); PINV(a_mg2); PINV(a_fs); PINV(a_ls); PINV(v_p1); PINS(s_p1); PINV(v_wo); PINS(s_wo); PINV(nsh);
  SCHED();
  PV_BODY(vs + 8)
#pragma unroll
  for (int e = 0; e < 4; ++e) *LDSP(f32x4, a_pw + 16 * e) = acc[e];
  if (isO) {
    const rsrc_t rwo = MKR(p_wo, 4096 * 1024 * 2);
#pragma unroll
    for (int i = 0; i < 8; ++i) wb[i] = LD128(rwo, v_wo + (i >> 2) * 2048, s_wo + 8 * (i & 3) * 2048, AUX_NT);
    SCHED();
  }
  BAR();
  if (w < 4) {
    f32x4 pv[8];
    const f32x4 f0 = *LDSP(f32x4, a_fs), f1 = *LDSP(f32x4, a_fs + 16);
    const unsigned lse = *LDSP(unsigned, a_ls);
#pragma unroll
    for (int ww = 0; ww < 8; ++ww) pv[ww] = *LDSP(f32x4, (ww < 4 ? a_mg : a_mg2) + (ww & 3) * 16384);
    SCHED();
    const v4u tb = tagbytes((tag & 0xF0F0F0F0u) | ((lse >> nsh) & 0x0F0F0F0Fu));
    const f32x4 o = ((f0[0] * pv[0] + f0[1] * pv[1]) + (f0[2] * pv[2] + f0[3] * pv[3])) + ((f1[0] * pv[4] + f1[1] * pv[5]) + (f1[2] * pv[6] + f1[3] * pv[7]));
    ST128(lowtag(o, tb), rws, v_p1, s_p1);
  }
  v4u tb = tagbytes(tag);
  int v_st = lane < 32 ? (n16 & 3) * 32 + g4 * 16 : OOR;
  int a_ln = LB + (n16 < 8 ? L_PART + ((4 * c8 + g4) * 8 + w) * 4 : L_DUMP + lane * 4);
  const bool odd4 = (g4 & 1) != 0;

  if (isO) {
    float ref = *(LAS float*)(L + L_LSE + (oi >> 2) * 4);
    int s_in = WS_P1 + (kvg & 1) * 524288 + oi * 16384 + w * 1024 + HOME(xg);
    int s_out = WS_P2 + (w & 3) * 131072 + (kvg >> 1) * 16384 + ((kvg & 1) * 32 + oi) * 128 + HOME(w);
    int a_t = LB + L_PART + ((8 * g4 + c8) * 8 + (n16 >> 3) * 4) * 4, a_l = LB + L_LSE + 32 + (n16 >> 3) * 16;
    if (lane == 63) a_ln = LB + L_LSE + 32 + w * 4;
    v_st += (n16 >> 2) * 32768;
    mkfrag<8>(wb, wa);
    PINV(ref); PINS(s_in); PINS(s_out); PINV(a_t); PINV(a_l); PINV(a_ln); PINV(v_st); PINV(tb);
    v4u pp; unsigned lb;
#pragma unroll 1
    for (unsigned sp = 0;; ++sp) {
      CFENCE();
      pp = LD128(rws, lane * 16, s_in, AUX_SC1);
      lb = lowbytes(pp);
      const unsigned long long miss = __builtin_amdgcn_ballot_w64(((lb ^ tag) & 0xF0F0F0F0u) != 0u);
      if (miss == 0ull || sp > SPIN_G) break;
      for (int k = __builtin_popcountll(miss); k > 16; k -= 8) __builtin_amdgcn_s_sleep(3);
    }
    lb &= 0x0F0F0F0Fu;
    const float ls = u2f(dppu<QEVEN>(lb) | (dppu<QODD>(lb) << 4));
    float ov, lg;
#define O_COMBINE() { \
      const float wgt = __builtin_amdgcn_exp2f(ls - ref); \
      const float o = linesum(wgt * val(pp.x), wgt * val(pp.y), wgt * val(pp.z), wgt * val(pp.w)); \
      float lw = tr32(wgt, wgt); lw = tr16(lw, lw); lw += dppf<ROR8>(lw); \
      *LDSP(float, a_ln) = lane == 63 ? lw : o; \
      BAR(); \
      const f32x4 t = *LDSP(f32x4, a_t), lq = *LDSP(f32x4, a_l); \
      ov = sum4(t); lg = sum4(lq); \
      ov += dppf<ROR8>(ov); lg += dppf<ROR8>(lg); }
    O_COMBINE()
    if (__builtin_expect(__any(!(lg < 1e15f)), 0)) {
      float mw = fmaxf(ls, dppf<ROR8>(ls)); mw = mx16(mw); mw = mx32(mw);
      if (lane == 0) *(LAS float*)(L + L_LSE + 64 + w * 4) = mw;
      BAR();
      const f32x4 m0 = *(LAS f32x4*)(L + L_LSE + 64), m1 = *(LAS f32x4*)(L + L_LSE + 80);
      ref = fmaxf(fmaxf(fmaxf(m0[0], m0[1]), fmaxf(m0[2], m0[3])), fmaxf(fmaxf(m1[0], m1[1]), fmaxf(m1[2], m1[3])));
      O_COMBINE()
    }
    ov *= frcp(lg);
    const unsigned op = pk2(dppf<QEVEN>(ov), dppf<QODD>(ov));
    const v4u af = {bcast16<0>(op), bcast16<2>(op), bcast16<4>(op), bcast16<6>(op)};
    f32x4 d[8];
#pragma unroll
    for (int e = 0; e < 8; ++e) d[e] = mfma(af, wa[e], ZF4);
    ST128(lowtag(pick4(d, odd4), tb), rws, v_st, s_out);
  } else if (isX) {
    float xcol = 0.f, gacol = 0.f, gbcol = 0.f, b2col = 0.f, c1col = 0.f, sbcol = 0.f;
    if (w == 0) {
#define XS_OK() __all((e0.y == tag) & (e1.y == tag) & (e2.y == tag) & (e3.y == tag))
      if (!XS_OK()) {
#pragma unroll 1
        for (unsigned sp = 0; sp < SPIN_G; ++sp) {
          CFENCE();
          X_LD()
          if (XS_OK()) break;
          __builtin_amdgcn_s_sleep(1);
        }
      }
      xcol = blo(e4); b2col = blo(e5);
      gacol = (lane & 1) ? bhi(e0.x) : blo(e0.x);
      sbcol = (lane & 1) ? bhi(e1.x) : blo(e1.x);
      c1col = rbf(1.f + ((lane & 1) ? bhi(e2.x) : blo(e2.x)));
      gbcol = (lane & 1) ? bhi(e3.x) : blo(e3.x);
    }
    int s_in = WS_P2 + (xi & 15) * 32768 + (w >> 2) * 16384 + (w & 3) * 2048 + HOME(xg);
    int a_t = LB + L_PART + ((lane & 31) * 8 + (lane >> 5) * 4) * 4;
    int v_v = r8 * 16 + c8 * 8192, s_v = WS_X1 + xi * 128;
    int v_m = n16 < 8 ? n16 * 8192 + g4 * 8 : OOR, s_m = WS_X1 + 4096 + xi * 32;
    PINV(gacol); PINV(gbcol); PINV(sbcol); PINV(c1col); PINV(xcol); PINV(b2col); PINV(xpiv);
    PINS(s_in); PINV(a_t); PINV(a_ln); PINV(v_v); PINS(s_v); PINV(v_m); PINS(s_m);
    v4u pa, pb;
#pragma unroll 1
    for (unsigned sp = 0;; ++sp) {
      CFENCE();
      pa = LD128(rws, lane * 16, s_in, AUX_SC1); pb = LD128(rws, lane * 16 + 1024, s_in, AUX_SC1);
      if (__all(((lowbytes(pa) ^ tag) | (lowbytes(pb) ^ tag)) == 0u) || sp > SPIN_G) break;
    }
    *LDSP(float, a_ln) = linesum(val(pa.x) + val(pb.x), val(pa.y) + val(pb.y), val(pa.z) + val(pb.z), val(pa.w) + val(pb.w));
    BAR();
    float x1col = 0.f;
    if (w == 0) {
      float at = sum4(*LDSP(f32x4, a_t));
      at = tr32(at, at);
      x1col = rbf(xcol + rbf(gacol * rbf(at)));
      const float xd = x1col - xpiv, xc = xd * c1col;
      const bool hi = lane >= 32;
      const float xh = hi ? xc - rbf(xc) : xc;
      const unsigned xp = nosent(pk2(dppf<QEVEN>(xh), dppf<QODD>(xh)));
#define BC2(i) (unsigned)__builtin_amdgcn_update_dpp((int)bcast16<2 * (i)>(xp), (int)xp, 0x150 + 8 + 2 * (i), 0xf, 0xc, false)
      ST128((v4u{BC2(0), BC2(1), BC2(2), BC2(3)}), rws, v_v, s_v);
      const float m0 = hi ? xc * xc : xd, m1 = hi ? xc * c1col : xd * xd, m2 = hi ? xc * sbcol : xc;
      const float r0 = rowsum16(tr16(m0, m1)), r1 = rowsum16(tr16(m2, xpiv));
      ST64((v2u{nosent(f2u(r0)), nosent(f2u(r1))}), rws, v_m, s_m);
      if (xi == 0 && lane == 0) ST32(tag, rws, 0, WS_EPOCH);
    }
    s_in = WS_P4 + (xi & 15) * 16384 + HOME(xg) + w * 1024;
    int v_in = w < 7 ? lane * 16 : OOR;
    unsigned tag4 = w < 7 ? tag : 0u;
    int v_o = lane < 32 ? lane * 2 : OOR;
    const rsrc_t rout = MKR(p_out, 2048);
    a_ln += n16 < 8 ? L_PART2 - L_PART : 0;
    PINS(s_in); PINV(v_in); PINS(tag4); PINV(v_o); PINV(a_ln);
#pragma unroll 1
    for (unsigned sp = 0;; ++sp) {
      CFENCE();
      pa = LD128(rws, v_in, s_in, AUX_SC1);
      if (__all(lowbytes(pa) == tag4) || sp > SPIN_G) break;
    }
    *LDSP(float, a_ln) = linesum(val(pa.x), val(pa.y), val(pa.z), val(pa.w));
    BAR();
    if (w == 0) {
      float mo = sum4(*LDSP(f32x4, a_t + (L_PART2 - L_PART)));
      mo = tr32(mo, mo);
      const float res = x1col + rbf(gbcol * rbf(rbf(mo) + b2col));
      __builtin_amdgcn_raw_buffer_store_b16((unsigned short)pk2(res, res), rout, v_o, xi * 64, AUX_SC1);
    }
  } else if (isM) {
    {
      const rsrc_t rw2 = MKR(p_w2, 3584 * 1024 * 2);
#pragma unroll
      for (int i = 0; i < 16; ++i) wb[i] = LD128(rw2, g4 * 16 * 2048 + n16 * 16, (64 * mi + i) * 2048 + w * 256, AUX_NT);
      SCHED();
    }
    float b1v = blo(e4);
    f32x4 uu = ZF4;
    float cs[5];
#pragma unroll
    for (int k = 0; k < 8; ++k) uu += *(LAS f32x4*)(L + L_V + k * 16384 + 8192 + lane * 16);
#pragma unroll
    for (int k = 0; k < 5; ++k) cs[k] = sum8(L, L_RED + k * 32);
    int s_in = WS_X1 + (xg & 3) * 16384 + HOME(xg) + w * 512, s_mm = WS_X1 + (xg & 3) * 16384 + HOME(xg) + 4096;
    int s_out = WS_P4 + (w & 3) * 65536 + mi * 128 + HOME(w);
    int v_in = g4 * 16 + (n16 & 1) * 64, a_w = LB + L_PART + (w >> 2) * 1024 + lane * 16 + (w & 3) * 4, a_r = LB + L_PART + lane * 16, a_co = LB + L_RED + 192;
    v_st += (n16 >> 2) * 16384;
    v4u w2f[16];
    mkfrag<16>(wb, w2f);
    PINV(uu); PINV(b1v); PINV(cs[0]); PINV(cs[1]); PINV(cs[2]); PINV(cs[3]); PINV(cs[4]);
    PINS(s_in); PINS(s_mm); PINS(s_out); PINV(v_in); PINV(a_w); PINV(a_r); PINV(a_co); PINV(v_st); PINV(tb);
    v4u xv[4];
    f32x4 d[8];
#define XV_LOAD() { _Pragma("unroll") for (int k = 0; k < 4; ++k) xv[k] = LD128(rws, v_in + k * 128, s_in, AUX_SC1); }
#define XV_MAX() umax(umax(mx4(xv[0]), mx4(xv[1])), umax(mx4(xv[2]), mx4(xv[3])))
#define W1_MFMA() { _Pragma("unroll") for (int c = 0; c < 4; ++c) d[c] = ZF4; \
      _Pragma("unroll") for (int k = 0; k < 4; ++k) _Pragma("unroll") for (int c = 0; c < 4; ++c) d[c] = mfma(xv[k], wa[4 * k + c], d[c]); }
#define W1_OUT() { PINV(d[0]); PINV(d[1]); PINV(d[2]); PINV(d[3]); \
      *LDSP(float, a_w) = (lane < 32 ? (odd4 ? d[1][0] : d[0][0]) : (odd4 ? d[3][0] : d[2][0])) + (lane < 32 ? (odd4 ? d[1][1] : d[0][1]) : (odd4 ? d[3][1] : d[2][1])); }
    if (w != MW) {
#pragma unroll 1
      for (unsigned sp = 0;; ++sp) {
        CFENCE();
        XV_LOAD()
        if (__all(XV_MAX() != SENT) || sp > SPIN_G) break;
      }
      W1_MFMA()
      W1_OUT()
    } else {
      v4u mm;
#pragma unroll 1
      for (unsigned sp = 0;; ++sp) {
        CFENCE();
        XV_LOAD()
        mm = LD128(rws, lane * 16, s_mm, AUX_SC1);
        if (__all(umax(XV_MAX(), mx4(mm)) != SENT) || sp > SPIN_G) break;
      }
      W1_MFMA()
      float t = linesum(u2f(mm.x), u2f(mm.y), u2f(mm.z), u2f(mm.w));
      t += dppf<ROR4>(t); t += dppf<ROR2>(t);
#define RL(n) u2f(__builtin_amdgcn_readlane(f2u(t), n))
      const float S1 = RL(0), S3 = RL(16), S2 = RL(32), S4 = RL(1), S6 = RL(17), S5 = RL(33), piv = RL(48) * (1.f / 1024.f);
      float m1, sd1, m2, sd2;
      ln_fin(piv, S1, S2, m1, sd1);
      m1 -= piv;
      const float A1 = fmaf(-m1, cs[0], S3), A2 = fmaf(m1, fmaf(m1, cs[1], -2.f * S5), S4), A3 = 2.f * fmaf(-m1, cs[4], S6);
      const float a1 = frcp(sd1);
      ln_fin(0.f, fmaf(a1, A1, cs[2]), fmaf(a1, fmaf(a1, A2, A3), cs[3]), m2, sd2);
      const float a2 = frcp(sd2), a12 = a1 * a2;
#pragma unroll
      for (int i = 0; i < 16; ++i) { __builtin_amdgcn_sched_group_barrier(0x8, 1, 0); __builtin_amdgcn_sched_group_barrier(0x2, 4, 0); }
      *LDSP(f32x4, a_co) = f32x4{a12, -a12 * m1, a2, -a2 * m2};
      W1_OUT()
    }
    BAR();
    const f32x4 co = *LDSP(f32x4, a_co), t0 = *LDSP(f32x4, a_r), t1 = *LDSP(f32x4, a_r + 1024);
    float z = fmaf(co[0], sum4(t0 + t1), fmaf(co[1], uu[0], fmaf(co[2], uu[1], co[3] * uu[2])));
    z = rbf(rbf(z) + b1v);
    const float hv = z * frcp(1.f + __builtin_amdgcn_exp2f(z * fmaf(z * z, -0.10294324f, -2.3022082f)));
    const unsigned hp = pk2(dppf<QEVEN>(hv), dppf<QODD>(hv));
    const v4u h0 = {bcast16<0>(hp), bcast16<2>(hp), bcast16<4>(hp), bcast16<6>(hp)}, h1 = {bcast16<8>(hp), bcast16<10>(hp), bcast16<12>(hp), bcast16<14>(hp)};
#pragma unroll
    for (int e = 0; e < 8; ++e) d[e] = mfma(h0, w2f[2 * e], ZF4);
#pragma unroll
    for (int e = 0; e < 8; ++e) d[e] = mfma(h1, w2f[2 * e + 1], d[e]);
    ST128(lowtag(pick4(d, odd4), tb), rws, v_st, s_out);
  }
}

extern "C" void kernel_launch(void* const* d_in, const int* in_sizes, int n_in, void* d_out, int out_size, void* d_ws, size_t ws_size,
                              hipStream_t stream) {
  void* ws = (void*)(((size_t)d_ws + ((size_t)2 << 20) - 1) & ~(((size_t)2 << 20) - 1));
  mk_kernel<<<256, 512, 0, stream>>>(d_in[0], d_in[1], d_in[2], d_in[3], d_in[4], d_in[6], d_in[7], d_in[8], d_in[9], d_in[10], d_in[11], d_out, ws);
}
```
